```python
import torch

def _softmax(x, axis=-1):
    return torch.softmax(x, dim=axis)
def _gelu(x):
    return torch.nn.functional.gelu(x, approximate="tanh")
def _silu(x):
    return torch.nn.functional.silu(x)
def _sigmoid(x):
    return torch.sigmoid(x)
def _layernorm(x, axis=-1, eps=1e-5):
    m = x.mean(dim=axis, keepdim=True)
    v = x.var(dim=axis, keepdim=True, unbiased=False)
    return (x - m) / torch.sqrt(v + eps)
def _scatter_add(tgt, idx, src, axis=0):
    return tgt.clone().index_add_(axis, idx, src)
def _segment_sum(src, seg, n):
    out = torch.zeros((n,) + src.shape[1:], dtype=src.dtype)
    return out.index_add_(0, seg, src)
def _pad(x, pw):
    flat = tuple(v for pair in reversed(pw) for v in pair)
    return torch.nn.functional.pad(x, flat)

kernel_name = 'standalone__sliding_block_cached__sampling'

def setup_inputs():

    _w = (lambda a: (lambda t: t.to(torch.bfloat16) if t.is_floating_point() else t)(torch.as_tensor(a)))
    B, Sq, D = 8, 8, 448
    Hq, Hkv, Sk, Dh = 16, 8, 8192, 256
    assert Hq % Hkv == 0, (Hq, Hkv)
    return {
        "x": _w((lambda *s: torch.randn(*s, dtype=torch.bfloat16) * 0.8)(B, Sq, D)),
        "k_cache": _w((lambda *s: torch.randn(*s, dtype=torch.bfloat16) * 0.8)(B, Hkv, Sk, Dh)),
        "v_cache": _w((lambda *s: torch.randn(*s, dtype=torch.bfloat16) * 0.05)(B, Hkv, Sk, Dh)),
        "wq": _w((lambda *s: torch.randn(*s, dtype=torch.bfloat16) * 0.8)(D, Hq * Dh) * (D ** -0.5)),
        "wo": _w((lambda *s: torch.randn(*s, dtype=torch.bfloat16) * 0.05)(Hq * Dh, D) * ((Hq * Dh) ** -0.5)),
    }

def reference(x, k_cache, v_cache, wq, wo):
    B, Sq, D = x.shape
    Hq, Dh = 16, 256
    Hkv, Sk = k_cache.shape[1], k_cache.shape[2]
    W = min(4096, max(256, Sk // 4))
    rep = Hq // Hkv
    q = (lambda x, *d: x.permute(*d))((x @ wq).reshape(B, Sq, Hq, Dh), 0, 2, 1, 3)
    k = (lambda x, n, axis: torch.repeat_interleave(x, n, dim=axis))(k_cache[:, :, Sk - W:, :], rep, axis=1)
    v = (lambda x, n, axis: torch.repeat_interleave(x, n, dim=axis))(v_cache[:, :, Sk - W:, :], rep, axis=1)
    scores = torch.einsum("bhqd,bhkd->bhqk", q, k) * (Dh ** -0.5)
    p = _softmax(scores, axis=-1)
    o = torch.einsum("bhqk,bhkd->bhqd", p, v)
    return (lambda x, *d: x.permute(*d))(o, 0, 2, 1, 3).reshape(B, Sq, Hq * Dh) @ wo
```

```cpp
#include <hip/hip_runtime.h>
#include <hip/hip_bf16.h>

constexpr int B = 8, SQ = 8, D = 448, HQ = 16, HKV = 8, SK = 8192, DH = 256, W = 2048;
typedef unsigned short u16;
__device__ __forceinline__ float bf2f(u16 x) { return __uint_as_float(((unsigned)x) << 16); }
__device__ __forceinline__ u16 f2bf(float f) { __hip_bfloat16 h = __float2bfloat16(f); return *(u16*)&h; }

__global__ void k_q(const u16* x, const u16* wq, float* q) {
  int n = blockIdx.x * 256 + threadIdx.x;
  int r = blockIdx.y;
  float acc = 0.f;
  for (int k = 0; k < D; ++k) acc += bf2f(x[r * D + k]) * bf2f(wq[(size_t)k * 4096 + n]);
  q[r * 4096 + n] = bf2f(f2bf(acc));
}
__global__ void k_attn(const float* q, const u16* kc, const u16* vc, float* o) {
  __shared__ float sq[256];
  __shared__ float p[W];
  __shared__ float red[256];
  int s = blockIdx.x, h = blockIdx.y, b = blockIdx.z, t = threadIdx.x;
  int kvh = h / 2;
  sq[t] = q[(b * SQ + s) * 4096 + h * DH + t];
  __syncthreads();
  const u16* kb = kc + ((size_t)(b * HKV + kvh) * SK + (SK - W)) * DH;
  const u16* vb = vc + ((size_t)(b * HKV + kvh) * SK + (SK - W)) * DH;
  float mx = -INFINITY;
  for (int j = t; j < W; j += 256) {
    float a = 0.f;
    for (int d = 0; d < DH; ++d) a += sq[d] * bf2f(kb[(size_t)j * DH + d]);
    a = bf2f(f2bf(a)) * 0.0625f;
    p[j] = a; mx = fmaxf(mx, a);
  }
  red[t] = mx; __syncthreads();
  for (int o2 = 128; o2 > 0; o2 >>= 1) { if (t < o2) red[t] = fmaxf(red[t], red[t + o2]); __syncthreads(); }
  mx = red[0]; __syncthreads();
  float sm = 0.f;
  for (int j = t; j < W; j += 256) { float e = __expf(p[j] - mx); p[j] = e; sm += e; }
  red[t] = sm; __syncthreads();
  for (int o2 = 128; o2 > 0; o2 >>= 1) { if (t < o2) red[t] += red[t + o2]; __syncthreads(); }
  sm = red[0];
  float inv = 1.f / sm;
  float acc = 0.f;
  for (int j = 0; j < W; ++j) acc += bf2f(f2bf(p[j] * inv)) * bf2f(vb[(size_t)j * DH + t]);
  o[(b * SQ + s) * 4096 + h * DH + t] = bf2f(f2bf(acc));
}
__global__ void k_o(const float* o, const u16* wo, u16* out) {
  int n = blockIdx.x * 64 + threadIdx.x;
  int r = blockIdx.y;
  float acc = 0.f;
  for (int k = 0; k < 4096; ++k) acc += o[r * 4096 + k] * bf2f(wo[(size_t)k * D + n]);
  out[r * D + n] = f2bf(acc);
}

extern "C" void kernel_launch(void* const* d_in, const int* in_sizes, int n_in,
                              void* d_out, int out_size, void* d_ws, size_t ws_size,
                              hipStream_t stream) {
  const u16* x = (const u16*)d_in[0];
  const u16* kc = (const u16*)d_in[1];
  const u16* vc = (const u16*)d_in[2];
  const u16* wq = (const u16*)d_in[3];
  const u16* wo = (const u16*)d_in[4];
  float* q = (float*)d_ws;
  float* o = q + 64 * 4096;
  k_q<<<dim3(16, 64), 256, 0, stream>>>(x, wq, q);
  k_attn<<<dim3(8, 16, 8), 256, 0, stream>>>(q, kc, vc, o);
  k_o<<<dim3(7, 64), 64, 0, stream>>>(o, wo, (u16*)d_out);
}
```

```cpp
#include <hip/hip_runtime.h>
#include <stdint.h>

typedef unsigned long long u64;
typedef unsigned u32x4 __attribute__((ext_vector_type(4)));
typedef unsigned u32x2 __attribute__((ext_vector_type(2)));
typedef float f32x4 __attribute__((ext_vector_type(4)));
typedef __bf16 bf16x8 __attribute__((ext_vector_type(8)));
typedef __bf16 bf16x2 __attribute__((ext_vector_type(2)));
typedef short s16x4 __attribute__((ext_vector_type(4)));
#define LAS __attribute__((address_space(3)))
#define GAS __attribute__((address_space(1)))

namespace {

constexpr int SK = 8192, WIN = 2048, DM = 448;
constexpr int TILE = 16384;
constexpr int NENT = 39;
constexpr int LWIN = 5;
constexpr int L_RING = 0;
constexpr int L_P = 131072;
constexpr int L_QP = L_P;
constexpr int L_QIMG = 147456;
constexpr int L_OFR = L_QIMG;
constexpr int L_RED = L_QIMG + 4096;
constexpr int L_WGT = 155648;
constexpr int L_MX = 157696;
constexpr int L_LS = 158208;
constexpr int L_FL = 158720;
constexpr int F_LAND = L_FL, F_REL = L_FL + 160, C_QSTG = L_FL + 324, C_QIMG = L_FL + 328, C_MX = L_FL + 332,
              C_LS = L_FL + 336, C_P = L_FL + 340, C_WGT = L_FL + 344;
constexpr int L_T = L_FL + 512;
constexpr int L_TOTAL = L_T + 16;
constexpr unsigned W_E = 0;
constexpr unsigned W_Q = 4096;
constexpr unsigned W_QC = W_Q + 512 * 1024;
constexpr unsigned W_LAM = W_QC + 8192;
constexpr unsigned W_H1 = W_LAM + 256 * 128;
constexpr unsigned H1_UNIT = 2176;
constexpr unsigned W_H2 = W_H1 + 64 * 16 * H1_UNIT;
constexpr unsigned H2_UNIT = 7680;
constexpr unsigned W_END = W_H2 + 256 * H2_UNIT;
constexpr unsigned OOB = 0x7fffffe0u;
constexpr unsigned SPIN_CAP = 1u << 16, LSPIN_CAP = 1u << 22;
constexpr unsigned PHI = 0x9E3779B9u;
constexpr int AUX_NT = 2, AUX_SC1 = 16;
constexpr float CSC = 0.0625f * 1.4426950408889634f;

#define ST(i) do { } while (0)

struct Params {
  const char* x; const char* kc; const char* vc; const char* wq; const char* wo; char* out; char* ws;
};

template <int CTRL> __device__ __forceinline__ unsigned dpp(unsigned v) { return __builtin_amdgcn_update_dpp(0u, v, CTRL, 0xf, 0xf, true); }
__device__ __forceinline__ unsigned row_sum(unsigned v) { v += dpp<0x128>(v); v += dpp<0x124>(v); v += dpp<0x122>(v); v += dpp<0x121>(v); return v; }
__device__ __forceinline__ unsigned row_xor(unsigned v) { v ^= dpp<0x128>(v); v ^= dpp<0x124>(v); v ^= dpp<0x122>(v); v ^= dpp<0x121>(v); return v; }
__device__ __forceinline__ unsigned oct_sum(unsigned v) { v += dpp<0xB1>(v); v += dpp<0x4E>(v); v += dpp<0x141>(v); return v; }
__device__ __forceinline__ unsigned oct_xor(unsigned v) { v ^= dpp<0xB1>(v); v ^= dpp<0x4E>(v); v ^= dpp<0x141>(v); return v; }
__device__ __forceinline__ unsigned sum16(unsigned v) { auto s = __builtin_amdgcn_permlane16_swap(v, v, false, false); return s[0] + s[1]; }
__device__ __forceinline__ unsigned sum32(unsigned v) { auto s = __builtin_amdgcn_permlane32_swap(v, v, false, false); return s[0] + s[1]; }
__device__ __forceinline__ unsigned xor16(unsigned v) { auto s = __builtin_amdgcn_permlane16_swap(v, v, false, false); return s[0] ^ s[1]; }
__device__ __forceinline__ unsigned xor32(unsigned v) { auto s = __builtin_amdgcn_permlane32_swap(v, v, false, false); return s[0] ^ s[1]; }
__device__ __forceinline__ unsigned wave_sum(unsigned v) { return sum32(sum16(row_sum(v))); }
__device__ __forceinline__ unsigned wave_xor(unsigned v) { return xor32(xor16(row_xor(v))); }
__device__ __forceinline__ float fmax16(float v) { auto s = __builtin_amdgcn_permlane16_swap(__float_as_uint(v), __float_as_uint(v), false, false); return fmaxf(__uint_as_float(s[0]), __uint_as_float(s[1])); }
__device__ __forceinline__ float fmax32(float v) { auto s = __builtin_amdgcn_permlane32_swap(__float_as_uint(v), __float_as_uint(v), false, false); return fmaxf(__uint_as_float(s[0]), __uint_as_float(s[1])); }
__device__ __forceinline__ float fadd16(float v) { auto s = __builtin_amdgcn_permlane16_swap(__float_as_uint(v), __float_as_uint(v), false, false); return __uint_as_float(s[0]) + __uint_as_float(s[1]); }
__device__ __forceinline__ float fadd32(float v) { auto s = __builtin_amdgcn_permlane32_swap(__float_as_uint(v), __float_as_uint(v), false, false); return __uint_as_float(s[0]) + __uint_as_float(s[1]); }
__device__ __forceinline__ unsigned pack2(float a, float b) { bf16x2 t = {(__bf16)a, (__bf16)b}; return __builtin_bit_cast(unsigned, t); }
__device__ __forceinline__ float bflo(unsigned v) { return __uint_as_float(v << 16); }
__device__ __forceinline__ float bfhi(unsigned v) { return __uint_as_float(v & 0xffff0000u); }
__device__ __forceinline__ unsigned xor4(u32x4 v) { return (v.x ^ v.y) ^ (v.z ^ v.w); }
__device__ __forceinline__ unsigned add4(u32x4 v) { return (v.x + v.y) + (v.z + v.w); }
__device__ __forceinline__ float fsel(unsigned m, float a, float b) { return __uint_as_float((__float_as_uint(a) & m) | (__float_as_uint(b) & ~m)); }
__device__ __forceinline__ unsigned seed4(unsigned k0) { return (k0 ^ (k0 + PHI)) ^ ((k0 + 2u * PHI) ^ (k0 + 3u * PHI)); }
__device__ __forceinline__ f32x4 mfma(bf16x8 a, bf16x8 b, f32x4 c) { return __builtin_amdgcn_mfma_f32_16x16x32_bf16(a, b, c, 0, 0, 0); }
__device__ __forceinline__ bf16x8 as_frag(u32x4 v) { return __builtin_bit_cast(bf16x8, v); }
__device__ __forceinline__ u32x4 lds_ld16(LAS char* base, unsigned off) { return *(LAS u32x4*)(base + off); }
__device__ __forceinline__ bf16x8 tr_pair(LAS char* base, unsigned off0, unsigned off1) {
  s16x4 a = __builtin_amdgcn_ds_read_tr16_b64_v4i16((LAS s16x4*)(base + off0));
  s16x4 b = __builtin_amdgcn_ds_read_tr16_b64_v4i16((LAS s16x4*)(base + off1));
  return __builtin_bit_cast(bf16x8, __builtin_shufflevector(a, b, 0, 1, 2, 3, 4, 5, 6, 7));
}
#define BARRIER_LDS() asm volatile("s_waitcnt lgkmcnt(0)\n\ts_barrier" ::: "memory")
template <int N> __device__ __forceinline__ void wait_vm() { asm volatile("s_waitcnt vmcnt(%0)" ::"n"(N) : "memory"); }
#define FENCE() asm volatile("" ::: "memory")

__device__ __forceinline__ void fl_set(unsigned addr, unsigned v) { asm volatile("ds_write_b32 %0, %1" ::"v"(addr), "v"(v) : "memory"); }
__device__ __forceinline__ void fl_inc(unsigned addr, int lane) {
  if (lane == 0) asm volatile("ds_add_u32 %0, %1" ::"v"(addr), "v"(1u) : "memory");
}
__device__ __forceinline__ void fl_add(unsigned addr, int lane) {
  asm volatile("s_waitcnt lgkmcnt(0)" ::: "memory");
  fl_inc(addr, lane);
}
__device__ __forceinline__ unsigned fl_get(unsigned addr) {
  unsigned r; asm volatile("ds_read_b32 %0, %1\n\ts_waitcnt lgkmcnt(0)" : "=v"(r) : "v"(addr) : "memory");
  return __builtin_amdgcn_readfirstlane(r);
}
__device__ __forceinline__ void fl_wait(unsigned addr, unsigned want) {
  for (unsigned n = 0; fl_get(addr) < want && n < LSPIN_CAP; ++n) { }
}
__device__ __forceinline__ void fl_wait_sleep(unsigned addr, unsigned want) {
  for (unsigned n = 0; fl_get(addr) < want && n < LSPIN_CAP; ++n) __builtin_amdgcn_s_sleep(1);
}

__device__ __forceinline__ int slot_of(int seq) { return seq & 7; }
__device__ __forceinline__ int seq_v(int t) { return 16 + t + ((t >> 1) < 7 ? (t >> 1) : 7); }
__device__ __forceinline__ int seq_w(int g) { return 18 + 3 * g; }
__device__ __forceinline__ bool is_w(int k) { return k % 3 == 2 && k < 21; }
__device__ __forceinline__ int v_of(int k) { return k < 21 ? k - (k + 1) / 3 : k - 7; }
__device__ __forceinline__ void dma16(__amdgpu_buffer_rsrc_t r, LAS char* lds, unsigned voff, unsigned soff) {
  __builtin_amdgcn_raw_ptr_buffer_load_lds(r, (LAS void*)lds, 16, voff, soff, 0, AUX_NT);
}

__global__ void __launch_bounds__(512, 2) attn_block_kernel(Params p) {
  extern __shared__ __attribute__((aligned(16))) char smem_[];
  LAS char* smem = (LAS char*)smem_;
  const int tid = threadIdx.x, lane = tid & 63;
  const int wave = __builtin_amdgcn_readfirstlane(tid >> 6);
  const int wg = blockIdx.x, xc = wg & 7, ii = wg >> 3;
  const int sp = (xc & 1) | ((xc >> 2) << 1);
  const int kvh = ((xc >> 1) & 1) | ((ii & 3) << 1);
  const int b = ii >> 2;
  const int rg = b >> 1, e8 = (b & 1) * 4 + sp;
  const int cluster = b * 8 + kvh, j32 = kvh * 4 + sp;
  const int q16 = lane & 15, g4 = lane >> 4;
  ST(0);

  const __amdgpu_buffer_rsrc_t rws = __builtin_amdgcn_make_buffer_rsrc(p.ws, 0, W_END, 0x00020000);
  const size_t kvbase = ((size_t)cluster * SK + (SK - WIN)) * 512;

  unsigned wof[16][4];

  auto read_w = [&](int seq) {
    const unsigned qq = (lane & 15) >> 2, pq = lane & 3, r = 4 * g4 + qq;
    const unsigned base = L_RING + slot_of(seq) * TILE + r * 128 + (pq & 1) * 8;
#pragma unroll
    for (int u = 0; u < 4; ++u) {
      LAS char* a = smem + (base + (((2 * u + (pq >> 1)) ^ (((r >> 1) & 3) << 1)) << 4));
#pragma unroll
      for (int ht = 0; ht < 4; ++ht) {
        const u32x4 f = __builtin_bit_cast(u32x4, tr_pair(a, ht * 4096, ht * 4096 + 2048));
        wof[ht * 4 + u][0] = f.x; wof[ht * 4 + u][1] = f.y; wof[ht * 4 + u][2] = f.z; wof[ht * 4 + u][3] = f.w;
      }
    }
  };

  if (wave == 4) {
    fl_set(L_FL + lane * 4, 0u);
    fl_set(L_FL + 256 + lane * 4, 0u);
  }

  if (wave >= 6) {
    const __amdgpu_buffer_rsrc_t rk = __builtin_amdgcn_make_buffer_rsrc((void*)(p.kc + kvbase), 0, WIN * 512, 0x00020000);
    const __amdgpu_buffer_rsrc_t rv = __builtin_amdgcn_make_buffer_rsrc((void*)(p.vc + kvbase), 0, WIN * 512, 0x00020000);
    const __amdgpu_buffer_rsrc_t rwo = __builtin_amdgcn_make_buffer_rsrc((void*)p.wo, 0, 4096 * 896, 0x00020000);
    const unsigned worow = (unsigned)((2 * kvh) * 256 + 64 * sp);
    const unsigned hh = lane >> 5, pp = lane & 31;
    const int hf = wave - 6;
    auto issue = [&](int s) {
      LAS char* dst = smem + L_RING + slot_of(s) * TILE + hf * 8192;
      const int k = s - 16;
      if (s < 16) {
        const unsigned so = (unsigned)(s * 4 + sp) * TILE + hf * 8192;
#pragma unroll
        for (int i = 0; i < 8; ++i) dma16(rk, dst + i * 1024, hh * 512 + ((pp ^ (2 * i + hh)) << 4), so + i * 1024);
      } else if (is_w(k)) {
        const unsigned r8 = lane >> 3, p8 = lane & 7;
        const unsigned vo = r8 * 896 + (k / 3) * 128 + ((p8 ^ (((r8 >> 1) & 3) << 1)) << 4);
#pragma unroll
        for (int n = 0; n < 8; ++n) dma16(rwo, dst + n * 1024, vo, (worow + hf * 256 + n * 8) * 896);
      } else {
        const unsigned so = (unsigned)(v_of(k) * 4 + sp) * TILE + hf * 8192;
#pragma unroll
        for (int i = 0; i < 8; ++i) dma16(rv, dst + i * 1024, hh * 512 + ((pp ^ (((2 * i + hh) & 7) << 1)) << 4), so + i * 1024);
      }
    };
#pragma unroll
    for (int s = 0; s < LWIN - 1; ++s) issue(s);
    BARRIER_LDS();
    for (int s = LWIN - 1; s < NENT; ++s) {
      const int prev = s - 8;
      if (s >= 8) fl_wait_sleep(F_REL + prev * 4, (prev >= 16 && !is_w(prev - 16)) ? 4u : 1u);
      issue(s);
      wait_vm<8 * (LWIN - 1)>();
      fl_inc(F_LAND + (s - (LWIN - 1)) * 4, lane);
      ST(12 + ((s - (LWIN - 1)) >> 1));
    }
    ST(1);
    static_assert(LWIN == 5, "the drain below announces the last LWIN - 1 entries");
    wait_vm<24>(); fl_inc(F_LAND + (NENT - 4) * 4, lane);
    wait_vm<16>(); fl_inc(F_LAND + (NENT - 3) * 4, lane);
    wait_vm<8>(); fl_inc(F_LAND + (NENT - 2) * 4, lane);
    wait_vm<0>(); fl_inc(F_LAND + (NENT - 1) * 4, lane);
    if (wave == 6) { fl_wait(F_LAND + seq_w(6) * 4, 2u); read_w(seq_w(6)); }
    else {
#pragma unroll
      for (int i = 0; i < 16; ++i) { wof[i][0] = 0u; wof[i][1] = 0u; wof[i][2] = 0u; wof[i][3] = 0u; }
    }
    ST(2);
  } else {
    u32x4 wraw[2][8], xf[2][2];
    u32x2 ep = {0u, 0u};
    if (wave < 4) {
      const __amdgpu_buffer_rsrc_t rx = __builtin_amdgcn_make_buffer_rsrc((void*)(p.x + rg * 16 * 896), 0, 16 * 896, 0x00020000);
      const __amdgpu_buffer_rsrc_t rq = __builtin_amdgcn_make_buffer_rsrc((void*)(p.wq + kvh * 1024 + e8 * 128), 0, 448 * 8192 - (kvh * 1024 + e8 * 128), 0x00020000);
      const unsigned vx = q16 * 896 + g4 * 16, vq = (32 * ((lane >> 3) & 1) + 8 * g4) * 8192 + (lane & 7) * 16;
#pragma unroll
      for (int mm = 0; mm < 2; ++mm) {
        const unsigned m = 2 * wave + mm;
        xf[mm][0] = __builtin_amdgcn_raw_buffer_load_b128(rx, m < 7 ? vx : OOB, m < 7 ? m * 128 : 0, 0);
        xf[mm][1] = __builtin_amdgcn_raw_buffer_load_b128(rx, m < 7 ? vx : OOB, m < 7 ? m * 128 + 64 : 0, 0);
      }
      ep = __builtin_amdgcn_raw_buffer_load_b64(rws, tid == 0 ? W_E + rg * 256 : OOB, 0, AUX_SC1);
#pragma unroll
      for (int mm = 0; mm < 2; ++mm) {
        const unsigned m = 2 * wave + mm;
#pragma unroll
        for (int j = 0; j < 8; ++j) wraw[mm][j] = __builtin_amdgcn_raw_buffer_load_b128(rq, m < 7 ? vq + j * 8192 : OOB, m < 7 ? m * 64 * 8192 : 0, AUX_NT);
      }
    }
    BARRIER_LDS();
    f32x4 sc[3][2];
    bf16x8 qf[8];
    auto load_q = [&]() {
#pragma unroll
      for (int ts = 0; ts < 8; ++ts) qf[ts] = as_frag(lds_ld16(smem, L_QIMG + q16 * 512 + (((4 * ts + g4) ^ q16) << 4)));
    };
    auto park_k = [&](int t, bf16x8 (&kf)[16]) {
      fl_wait(F_LAND + t * 4, 2u);
      const unsigned so = L_RING + slot_of(t) * TILE + q16 * 512;
#pragma unroll
      for (int i = 0; i < 4; ++i) {
        LAS char* kp = smem + (so + (((4 * i + g4) ^ q16) << 4));
        kf[i] = as_frag(lds_ld16(kp, 0)); kf[8 + i] = as_frag(lds_ld16(kp, 8192));
        kf[4 + i] = as_frag(lds_ld16(kp, 256)); kf[12 + i] = as_frag(lds_ld16(kp, 8192 + 256));
      }
      fl_add(F_REL + t * 4, lane);
    };
    auto qk = [&](const bf16x8 (&kf)[16], f32x4 (&s)[2]) {
      f32x4 s0 = {0.f, 0.f, 0.f, 0.f}, s1 = {0.f, 0.f, 0.f, 0.f};
#pragma unroll
      for (int i = 0; i < 8; ++i) { s0 = mfma(kf[i], qf[i], s0); s1 = mfma(kf[8 + i], qf[i], s1); }
      s[0] = s0; s[1] = s1;
    };
    if (wave < 4) {
      {
        f32x4 a0[8], a1[8];
#pragma unroll
        for (int u = 0; u < 8; ++u) { a0[u] = f32x4{0.f, 0.f, 0.f, 0.f}; a1[u] = f32x4{0.f, 0.f, 0.f, 0.f}; }
#pragma unroll
        for (int mm = 0; mm < 2; ++mm)
#pragma unroll
          for (int u = 0; u < 8; ++u) {
            u32x4 bq;
#pragma unroll
            for (int pp = 0; pp < 4; ++pp)
              bq[pp] = __builtin_amdgcn_perm(wraw[mm][2 * pp + 1][u >> 1], wraw[mm][2 * pp][u >> 1], (u & 1) ? 0x07060302u : 0x05040100u);
            a0[u] = mfma(as_frag(xf[mm][0]), as_frag(bq), a0[u]);
            a1[u] = mfma(as_frag(xf[mm][1]), as_frag(bq), a1[u]);
          }
        const bool hx = (lane & 8) != 0;
        const unsigned mx8 = hx ? 0xffffffffu : 0u;
        float r[8][4];
#pragma unroll
        for (int u = 0; u < 8; ++u)
#pragma unroll
          for (int i = 0; i < 4; ++i) { const float own = fsel(mx8, a1[u][i], a0[u][i]); r[u][i] = own + __uint_as_float(dpp<0x128>(__float_as_uint(own))); }
#pragma unroll
        for (int k = 0; k < 2; ++k) {
          LAS char* d = smem + (L_QP + wave * 4096 + (4 * g4 + (hx ? 2 : 0) + k) * 256 + (lane & 7) * 32);
          f32x4 lo, hi;
#pragma unroll
          for (int u = 0; u < 4; ++u) { lo[u] = fsel(mx8, r[u][2 + k], r[u][k]); hi[u] = fsel(mx8, r[4 + u][2 + k], r[4 + u][k]); }
          *(LAS f32x4*)d = lo; *(LAS f32x4*)(d + 16) = hi;
        }
        if (tid == 0) {
          const unsigned tlo = ep.x + 1u, thi = ep.y + ((tlo == 0u) ? 1u : 0u);
          *(LAS u32x4*)(smem + L_T) = u32x4{tlo, thi, tlo, (tlo * 0x9E3779B1u) ^ thi};
        }
      }
      fl_add(C_QSTG, lane);
      ST(1);
      if (wave < 2) {
        fl_wait(C_QSTG, 4u);
        const u32x4 tw = *(LAS u32x4*)(smem + L_T);
        const unsigned seedA = __builtin_amdgcn_readfirstlane(tw.z), seedB = __builtin_amdgcn_readfirstlane(tw.w);
        LAS char* d = smem + (L_QP + (8 * wave + (lane >> 3)) * 256 + (lane & 7) * 32);
        f32x4 lo = *(LAS f32x4*)d, hi = *(LAS f32x4*)(d + 16);
#pragma unroll
        for (int w = 1; w < 4; ++w) { lo += *(LAS f32x4*)(d + w * 4096); hi += *(LAS f32x4*)(d + w * 4096 + 16); }
        const u32x4 v = {pack2(lo[0], lo[1]), pack2(lo[2], lo[3]), pack2(hi[0], hi[1]), pack2(hi[2], hi[3])};
        const unsigned piece = ((2 * rg + wave) * 8 + kvh) * 8 + e8;
        __builtin_amdgcn_raw_buffer_store_b128(v, rws, W_Q + piece * 1024 + lane * 16, 0, AUX_SC1);
        const unsigned a = wave_sum(add4(v)) + seedA;
        const unsigned x = wave_xor(xor4(v)) ^ (seedB + e8 * PHI);
        __builtin_amdgcn_raw_buffer_store_b128(u32x4{a, x, seedA, 0u}, rws, lane == 0 ? W_QC + piece * 16 : OOB, 0, AUX_SC1);
      }
      ST(2);
      bf16x8 k0[16], k1[16];
      park_k(4 + wave, k0);
      park_k(8 + wave, k1);
      ST(3);
      fl_wait(C_QIMG, 2u);
      load_q();
      qk(k0, sc[0]);
      qk(k1, sc[1]);
      park_k(12 + wave, k0);
      qk(k0, sc[2]);
    } else {
      bf16x8 ka[16], kb[16];
      park_k(wave - 4, ka);
      park_k(wave - 2, kb);
      ST(2);
      fl_wait_sleep(C_QSTG, 4u);
      const u32x4 tw = *(LAS u32x4*)(smem + L_T);
      const unsigned seedA = __builtin_amdgcn_readfirstlane(tw.z), seedB = __builtin_amdgcn_readfirstlane(tw.w);
      const unsigned p0 = 4 * (wave - 4), pbase = (b * 8 + kvh) * 8 + p0, sxq = seed4(seedB + p0 * PHI);
      u32x4 v[4];
      for (unsigned spins = 0;; ++spins) {
        FENCE();
#pragma unroll
        for (int k = 0; k < 4; ++k) v[k] = __builtin_amdgcn_raw_buffer_load_b128(rws, W_Q + (pbase + k) * 1024 + lane * 16, 0, AUX_SC1);
        const u32x2 c = __builtin_amdgcn_raw_buffer_load_b64(rws, lane < 4 ? W_QC + (pbase + lane) * 16 : OOB, 0, AUX_SC1);
        const unsigned a = wave_sum((add4(v[0]) + add4(v[1])) + (add4(v[2]) + add4(v[3])) - c.x);
        const unsigned x = wave_xor((xor4(v[0]) ^ xor4(v[1])) ^ (xor4(v[2]) ^ xor4(v[3])) ^ c.y);
        const bool ok = (a == 0u - 4u * seedA) && (x == sxq);
        if (__builtin_amdgcn_readfirstlane(ok) || spins > SPIN_CAP) break;
        __builtin_amdgcn_s_sleep(1);
      }
#pragma unroll
      for (int k = 0; k < 4; ++k) {
        const unsigned row = (wave - 4) * 8 + (lane >> 3), c16 = k * 8 + (lane & 7);
        *(LAS u32x4*)(smem + L_QIMG + row * 512 + ((c16 ^ row) << 4)) = v[k];
      }
      fl_add(C_QIMG, lane);
      ST(3);
      fl_wait(C_QIMG, 2u);
      load_q();
      qk(ka, sc[0]);
      qk(kb, sc[1]);
      sc[2][0] = f32x4{-INFINITY, -INFINITY, -INFINITY, -INFINITY}; sc[2][1] = sc[2][0];
    }
    ST(4);
    float mx = -INFINITY;
#pragma unroll
    for (int i = 0; i < 3; ++i)
#pragma unroll
      for (int k = 0; k < 2; ++k) mx = fmaxf(mx, fmaxf(fmaxf(sc[i][k][0], sc[i][k][1]), fmaxf(sc[i][k][2], sc[i][k][3])));
    mx = fmax32(fmax16(mx));
    if (lane < 16) *(LAS float*)(smem + L_MX + wave * 64 + lane * 4) = mx;
    fl_add(C_MX, lane);
    fl_wait(C_MX, 6u);
    {
      LAS float* pm = (LAS float*)(smem + L_MX + q16 * 4);
      mx = fmaxf(fmaxf(fmaxf(pm[0], pm[16]), fmaxf(pm[32], pm[48])), fmaxf(pm[64], pm[80]));
    }
    const float mc = mx * CSC;
    float ls = 0.f;
#pragma unroll
    for (int i = 0; i < 3; ++i)
#pragma unroll
      for (int k = 0; k < 2; ++k)
#pragma unroll
        for (int e = 0; e < 4; ++e) { const float pe = __builtin_amdgcn_exp2f(sc[i][k][e] * CSC - mc); sc[i][k][e] = pe; ls += pe; }
    ls = fadd32(fadd16(ls));
    if (lane < 16) *(LAS float*)(smem + L_LS + wave * 64 + lane * 4) = ls;
    fl_add(C_LS, lane);
    fl_wait(C_LS, 6u);
    {
      LAS float* pl = (LAS float*)(smem + L_LS + q16 * 4);
      ls = ((pl[0] + pl[16]) + (pl[32] + pl[48])) + (pl[64] + pl[80]);
    }
    const float inv = 1.0f / ls;
#pragma unroll
    for (int i = 0; i < 3; ++i) {
      const int t = wave < 4 ? 4 + 4 * i + wave : (wave - 4) + 2 * i;
      if (wave < 4 || i < 2)
        *(LAS u32x4*)(smem + L_P + t * 1024 + lane * 16) =
            u32x4{pack2(sc[i][0][0] * inv, sc[i][0][1] * inv), pack2(sc[i][0][2] * inv, sc[i][0][3] * inv),
                  pack2(sc[i][1][0] * inv, sc[i][1][1] * inv), pack2(sc[i][1][2] * inv, sc[i][1][3] * inv)};
    }
    fl_add(C_P, lane);
    const u32x4 tw = *(LAS u32x4*)(smem + L_T);
    const unsigned seedA = __builtin_amdgcn_readfirstlane(tw.z), seedB = __builtin_amdgcn_readfirstlane(tw.w);
    if (wave == 4) {
      const unsigned lamb = __float_as_uint(mc + __builtin_amdgcn_logf(ls));
      const unsigned a = row_sum(lamb) + seedA, x = row_xor(lamb) ^ (seedB + sp * PHI);
      const unsigned hb = W_LAM + (cluster * 4 + sp) * 128;
      __builtin_amdgcn_raw_buffer_store_b32(lamb, rws, lane < 16 ? hb + lane * 4 : OOB, 0, AUX_SC1);
      __builtin_amdgcn_raw_buffer_store_b64(u32x2{a, x}, rws, lane == 0 ? hb + 64 : OOB, 0, AUX_SC1);
    }
    ST(5);
    if (wave < 4) {
      unsigned vad[4];
#pragma unroll
      for (int i = 0; i < 4; ++i) {
        const unsigned qq = (lane & 15) >> 2, pq = lane & 3, r = 4 * g4 + qq;
        vad[i] = L_RING + r * 512 + (((8 * wave + 2 * i + (pq >> 1)) ^ ((r & 7) << 1)) << 4) + (pq & 1) * 8;
      }
      f32x4 o[4];
#pragma unroll
      for (int j = 0; j < 4; ++j) o[j] = f32x4{0.f, 0.f, 0.f, 0.f};
      fl_wait(C_P, 6u);
      for (int t = 0; t < 16; ++t) {
        const int sq = seq_v(t);
        const u32x4 pf = lds_ld16(smem, L_P + t * 1024 + lane * 16);
        fl_wait(F_LAND + sq * 4, 2u);
        const unsigned so = slot_of(sq) * TILE;
        bf16x8 vf[4];
#pragma unroll
        for (int j = 0; j < 4; ++j) vf[j] = tr_pair(smem + (vad[j] + so), 0, 8192);
        fl_add(F_REL + sq * 4, lane);
#pragma unroll
        for (int j = 0; j < 4; ++j) o[j] = mfma(vf[j], as_frag(pf), o[j]);
        if (t == 2 * wave + 2) { fl_wait(F_LAND + seq_w(wave) * 4, 2u); read_w(seq_w(wave)); fl_add(F_REL + seq_w(wave) * 4, lane); }
      }
      ST(6);
      const unsigned ub = W_H1 + ((cluster * 4 + wave) * 4 + sp) * H1_UNIT;
      const u32x4 v0 = {pack2(o[0][0], o[0][1]), pack2(o[0][2], o[0][3]), pack2(o[1][0], o[1][1]), pack2(o[1][2], o[1][3])};
      const u32x4 v1 = {pack2(o[2][0], o[2][1]), pack2(o[2][2], o[2][3]), pack2(o[3][0], o[3][1]), pack2(o[3][2], o[3][3])};
      __builtin_amdgcn_raw_buffer_store_b128(v0, rws, ub + lane * 16, 0, AUX_SC1);
      __builtin_amdgcn_raw_buffer_store_b128(v1, rws, ub + 1024 + lane * 16, 0, AUX_SC1);
      const unsigned a = oct_sum(add4(v0) + add4(v1)) + seedA;
      const unsigned x = oct_xor(xor4(v0) ^ xor4(v1)) ^ (seedB + sp * PHI);
      __builtin_amdgcn_raw_buffer_store_b128(u32x4{a, x, seedA, 0u}, rws, (lane & 7) == 0 ? ub + 2048 + (lane >> 3) * 16 : OOB, 0, AUX_SC1);
    } else {
      fl_wait_sleep(F_LAND + seq_w(wave) * 4, 2u);
      read_w(seq_w(wave));
      fl_add(F_REL + seq_w(wave) * 4, lane);
      if (wave == 5) {
        const unsigned hb = W_LAM + cluster * 4 * 128, sx4 = seed4(seedB);
        unsigned lam;
        for (unsigned spins = 0;; ++spins) {
          FENCE();
          lam = __builtin_amdgcn_raw_buffer_load_b32(rws, hb + g4 * 128 + q16 * 4, 0, AUX_SC1);
          const u32x2 c = __builtin_amdgcn_raw_buffer_load_b64(rws, lane < 4 ? hb + lane * 128 + 64 : OOB, 0, AUX_SC1);
          const bool ok = (wave_sum(lam - c.x) == 0u - 4u * seedA) && (wave_xor(lam ^ c.y) == sx4);
          if (__builtin_amdgcn_readfirstlane(ok) || spins > SPIN_CAP) break;
          __builtin_amdgcn_s_sleep(8);
        }
        const float l = __uint_as_float(lam);
        const float w = __builtin_amdgcn_exp2f(l - fmax32(fmax16(l)));
        *(LAS float*)(smem + L_WGT + q16 * 16 + g4 * 4) = w / fadd32(fadd16(w));
        fl_add(C_WGT, lane);
      }
    }
  }
  ST(7);

  const u32x4 tw = *(LAS u32x4*)(smem + L_T);
  const unsigned seedA = __builtin_amdgcn_readfirstlane(tw.z), seedB = __builtin_amdgcn_readfirstlane(tw.w);
  {
    const int gg = wave >> 1, hf = wave & 1;
    const int tt = lane >> 5, s8 = (lane >> 2) & 7, c4 = lane & 3;
    fl_wait(C_WGT, 1u);
    const f32x4 wv = *(LAS f32x4*)(smem + L_WGT + (hf * 8 + s8) * 16);
    ST(8);
    const unsigned ub = W_H1 + (cluster * 4 + sp) * 4 * H1_UNIT, sx4 = seed4(seedB);
    const unsigned po = tt * 1024 + (gg * 16 + hf * 8 + s8) * 16 + c4 * 4;
    const unsigned ca = lane < 4 ? ub + lane * H1_UNIT + 2048 + (gg * 2 + hf) * 16 : OOB;
    unsigned pay[4] = {0u, 0u, 0u, 0u};
    unsigned need = 0xFu, seen = 0u;
    for (unsigned spins = 0;; ++spins) {
      FENCE();
      const u32x4 c = __builtin_amdgcn_raw_buffer_load_b128(rws, ca, 0, AUX_SC1);
      unsigned np[4];
#pragma unroll
      for (int s = 0; s < 4; ++s) np[s] = __builtin_amdgcn_raw_buffer_load_b32(rws, (need >> s) & 1u ? ub + s * H1_UNIT + po : OOB, 0, AUX_SC1);
#pragma unroll
      for (int s = 0; s < 4; ++s) pay[s] = (need >> s) & 1u ? np[s] : pay[s];
      const unsigned now = (unsigned)__ballot(c.z == seedA) & 0xFu;
      if (now == 0xFu) {
        const unsigned a = wave_sum((pay[0] + pay[1]) + (pay[2] + pay[3]) - c.x);
        const unsigned x = wave_xor((pay[0] ^ pay[1]) ^ (pay[2] ^ pay[3]) ^ c.y);
        const bool ok = (a == 0u - 4u * seedA) && (x == sx4);
        if (__builtin_amdgcn_readfirstlane(ok) || spins > SPIN_CAP) break;
        need = 0xFu;
      } else {
        if (spins > SPIN_CAP) break;
        need = ~(now & seen) & 0xFu;
        __builtin_amdgcn_s_sleep(2);
      }
      seen = now;
    }
    const float olo = (wv[0] * bflo(pay[0]) + wv[1] * bflo(pay[1])) + (wv[2] * bflo(pay[2]) + wv[3] * bflo(pay[3]));
    const float ohi = (wv[0] * bfhi(pay[0]) + wv[1] * bfhi(pay[1])) + (wv[2] * bfhi(pay[2]) + wv[3] * bfhi(pay[3]));
    const unsigned ov = pack2(olo, ohi);
    LAS unsigned* dst = (LAS unsigned*)(smem + L_OFR + hf * 2048 + tt * 1024 + (gg * 16 + s8) * 16 + c4 * 4);
    dst[0] = ov; dst[32] = ov;
  }
  ST(9);
  BARRIER_LDS();
  const unsigned sb = W_H2 + (b * 32 + j32) * H2_UNIT;
  if (wave < 7) {
    bf16x8 of[4];
#pragma unroll
    for (int ht = 0; ht < 4; ++ht) of[ht] = as_frag(lds_ld16(smem + (L_OFR + lane * 16), ht * 1024));
    __builtin_amdgcn_sched_barrier(0);
    f32x4 c[4];
#pragma unroll
    for (int u = 0; u < 4; ++u) c[u] = f32x4{0.f, 0.f, 0.f, 0.f};
#pragma unroll
    for (int ht = 0; ht < 4; ++ht)
#pragma unroll
      for (int u = 0; u < 4; ++u) c[u] = mfma(as_frag(u32x4{wof[ht * 4 + u][0], wof[ht * 4 + u][1], wof[ht * 4 + u][2], wof[ht * 4 + u][3]}), of[ht], c[u]);
    const unsigned msk = q16 >= 8 ? 0xffffffffu : 0u;
    const u32x4 lo4 = {pack2(c[0][0], c[0][1]), pack2(c[0][2], c[0][3]), pack2(c[1][0], c[1][1]), pack2(c[1][2], c[1][3])};
    const u32x4 hi4 = {pack2(c[2][0], c[2][1]), pack2(c[2][2], c[2][3]), pack2(c[3][0], c[3][1]), pack2(c[3][2], c[3][3])};
    const u32x4 v = (hi4 & msk) | (lo4 & ~msk);
    __builtin_amdgcn_raw_buffer_store_b128(v, rws, sb + wave * 1024 + lane * 16, 0, AUX_SC1);
    const unsigned a = row_sum(add4(v)) + seedA;
    const unsigned x = row_xor(xor4(v)) ^ (seedB + j32 * PHI);
    __builtin_amdgcn_raw_buffer_store_b128(u32x4{a, x, seedA, 0u}, rws, q16 == 0 ? sb + 7168 + (wave * 4 + g4) * 16 : OOB, 0, AUX_SC1);
  }
  ST(10);
  if (j32 < 28) {
    const int G = j32 >> 2, g = j32 & 3;
    const unsigned bb = W_H2 + b * 32 * H2_UNIT;
    const unsigned po = bb + (4 * wave + g4) * H2_UNIT + G * 1024 + (g * 16 + q16) * 16;
    const unsigned sx = seed4(seedB + (4 * wave) * PHI);
    const unsigned ca = lane < 4 ? bb + (4 * wave + lane) * H2_UNIT + 7168 + j32 * 16 : OOB;
    for (unsigned spins = 0; spins < SPIN_CAP; ++spins) {
      FENCE();
      const unsigned cx = __builtin_amdgcn_raw_buffer_load_b32(rws, ca + 8, 0, AUX_SC1);
      if (!__any(lane < 4 && cx != seedA)) break;
      __builtin_amdgcn_s_sleep(2);
    }
    u32x4 v;
    for (unsigned spins = 0;; ++spins) {
      FENCE();
      const u32x4 c = __builtin_amdgcn_raw_buffer_load_b128(rws, ca, 0, AUX_SC1);
      v = __builtin_amdgcn_raw_buffer_load_b128(rws, po, 0, AUX_SC1);
      const unsigned a = wave_sum(add4(v) - c.x);
      const unsigned x = wave_xor(xor4(v) ^ c.y);
      const bool ok = (a == 0u - 4u * seedA) && (x == sx);
      if (__builtin_amdgcn_readfirstlane(ok) || spins > SPIN_CAP) break;
      __builtin_amdgcn_s_sleep(1);
    }
    f32x4 s0 = {bflo(v.x), bfhi(v.x), bflo(v.y), bfhi(v.y)}, s1 = {bflo(v.z), bfhi(v.z), bflo(v.w), bfhi(v.w)};
#pragma unroll
    for (int u = 0; u < 4; ++u) { s0[u] = fadd32(fadd16(s0[u])); s1[u] = fadd32(fadd16(s1[u])); }
    if (lane < 16) { *(LAS f32x4*)(smem + L_RED + wave * 512 + lane * 32) = s0; *(LAS f32x4*)(smem + L_RED + wave * 512 + lane * 32 + 16) = s1; }
    ST(11);
    BARRIER_LDS();
    if (wave == 0 && lane < 32) {
      LAS char* rp = smem + (L_RED + q16 * 32 + (lane >> 4) * 16);
      f32x4 t = *(LAS f32x4*)rp;
#pragma unroll
      for (int w = 1; w < 8; ++w) t += *(LAS f32x4*)(rp + w * 512);
      const int col = 64 * G + 16 * (2 * (q16 >> 3) + (lane >> 4)) + 4 * g;
      *(GAS u32x2*)(p.out + ((size_t)(b * 8 + (q16 & 7)) * DM + col) * 2) = u32x2{pack2(t[0], t[1]), pack2(t[2], t[3])};
      if (j32 == 0 && (b & 1) == 0 && lane == 0)
        __hip_atomic_store((GAS u64*)(p.ws + W_E + rg * 256), ((u64)tw.y << 32) | tw.x, __ATOMIC_RELAXED, __HIP_MEMORY_SCOPE_AGENT);
    }
  }
  ST(31);
}

}

extern "C" void kernel_launch(void* const* d_in, const int* in_sizes, int n_in, void* d_out, int out_size, void* d_ws,
                              size_t ws_size, hipStream_t stream) {
  Params p;
  p.x = (const char*)d_in[0];
  p.kc = (const char*)d_in[1];
  p.vc = (const char*)d_in[2];
  p.wq = (const char*)d_in[3];
  p.wo = (const char*)d_in[4];
  p.out = (char*)d_out;
  p.ws = (char*)d_ws;
  (void)hipFuncSetAttribute((const void*)attn_block_kernel, hipFuncAttributeMaxDynamicSharedMemorySize, L_TOTAL);
  hipLaunchKernelGGL(attn_block_kernel, dim3(256), dim3(512), L_TOTAL, stream, p);
}
```

```cpp
#define AUXST 16
#include <hip/hip_runtime.h>
#include <stdint.h>

typedef unsigned long long u64;
typedef unsigned u32x4 __attribute__((ext_vector_type(4)));
typedef unsigned u32x2 __attribute__((ext_vector_type(2)));
typedef float f32x4 __attribute__((ext_vector_type(4)));
typedef __bf16 bf16x8 __attribute__((ext_vector_type(8)));
typedef __bf16 bf16x2 __attribute__((ext_vector_type(2)));
typedef short s16x4 __attribute__((ext_vector_type(4)));
#define LAS __attribute__((address_space(3)))
#define GAS __attribute__((address_space(1)))

namespace {

constexpr int SK = 8192, WIN = 2048, DM = 448;
constexpr int TILE = 16384;
constexpr int NENT = 39;
constexpr int LWIN = 5;
constexpr int L_RING = 0;
constexpr int L_P = 131072;
constexpr int L_QP = L_P;
constexpr int L_QIMG = 147456;
constexpr int L_OFR = L_QIMG;
constexpr int L_RED = L_QIMG + 4096;
constexpr int L_WGT = 155648;
constexpr int L_MX = 157696;
constexpr int L_LS = 158208;
constexpr int L_FL = 158720;
constexpr int F_LAND = L_FL, F_REL = L_FL + 160, C_QSTG = L_FL + 324, C_QIMG = L_FL + 328, C_MX = L_FL + 332,
              C_LS = L_FL + 336, C_P = L_FL + 340, C_WGT = L_FL + 344;
constexpr int L_T = L_FL + 512;
constexpr int L_TOTAL = L_T + 16;
constexpr unsigned W_E = 0;
constexpr unsigned W_Q = 4096;
constexpr unsigned W_QC = W_Q + 512 * 1024;
constexpr unsigned W_LAM = W_QC + 8192;
constexpr unsigned W_H1 = W_LAM + 256 * 128;
constexpr unsigned H1_UNIT = 2176;
constexpr unsigned W_H2 = W_H1 + 64 * 16 * H1_UNIT;
constexpr unsigned H2_UNIT = 7680;
constexpr unsigned W_END = W_H2 + 256 * H2_UNIT;
constexpr unsigned OOB = 0x7fffffe0u;
constexpr unsigned SPIN_CAP = 1u << 16, LSPIN_CAP = 1u << 22;
constexpr unsigned PHI = 0x9E3779B9u;
constexpr int AUX_NT = 2, AUX_SC1 = 16, AUX_ST = AUXST;
constexpr float CSC = 0.0625f * 1.4426950408889634f;

#define ST(i) do { } while (0)

struct Params {
  const char* x; const char* kc; const char* vc; const char* wq; const char* wo; char* out; char* ws;
};

template <int CTRL> __device__ __forceinline__ unsigned dpp(unsigned v) { return __builtin_amdgcn_update_dpp(0u, v, CTRL, 0xf, 0xf, true); }
__device__ __forceinline__ unsigned row_sum(unsigned v) { v += dpp<0x128>(v); v += dpp<0x124>(v); v += dpp<0x122>(v); v += dpp<0x121>(v); return v; }
__device__ __forceinline__ unsigned row_xor(unsigned v) { v ^= dpp<0x128>(v); v ^= dpp<0x124>(v); v ^= dpp<0x122>(v); v ^= dpp<0x121>(v); return v; }
__device__ __forceinline__ unsigned oct_sum(unsigned v) { v += dpp<0xB1>(v); v += dpp<0x4E>(v); v += dpp<0x141>(v); return v; }
__device__ __forceinline__ unsigned oct_xor(unsigned v) { v ^= dpp<0xB1>(v); v ^= dpp<0x4E>(v); v ^= dpp<0x141>(v); return v; }
__device__ __forceinline__ unsigned sum16(unsigned v) { auto s = __builtin_amdgcn_permlane16_swap(v, v, false, false); return s[0] + s[1]; }
__device__ __forceinline__ unsigned sum32(unsigned v) { auto s = __builtin_amdgcn_permlane32_swap(v, v, false, false); return s[0] + s[1]; }
__device__ __forceinline__ unsigned xor16(unsigned v) { auto s = __builtin_amdgcn_permlane16_swap(v, v, false, false); return s[0] ^ s[1]; }
__device__ __forceinline__ unsigned xor32(unsigned v) { auto s = __builtin_amdgcn_permlane32_swap(v, v, false, false); return s[0] ^ s[1]; }
__device__ __forceinline__ unsigned wave_sum(unsigned v) { return sum32(sum16(row_sum(v))); }
__device__ __forceinline__ unsigned wave_xor(unsigned v) { return xor32(xor16(row_xor(v))); }
__device__ __forceinline__ float fmax16(float v) { auto s = __builtin_amdgcn_permlane16_swap(__float_as_uint(v), __float_as_uint(v), false, false); return fmaxf(__uint_as_float(s[0]), __uint_as_float(s[1])); }
__device__ __forceinline__ float fmax32(float v) { auto s = __builtin_amdgcn_permlane32_swap(__float_as_uint(v), __float_as_uint(v), false, false); return fmaxf(__uint_as_float(s[0]), __uint_as_float(s[1])); }
__device__ __forceinline__ float fadd16(float v) { auto s = __builtin_amdgcn_permlane16_swap(__float_as_uint(v), __float_as_uint(v), false, false); return __uint_as_float(s[0]) + __uint_as_float(s[1]); }
__device__ __forceinline__ float fadd32(float v) { auto s = __builtin_amdgcn_permlane32_swap(__float_as_uint(v), __float_as_uint(v), false, false); return __uint_as_float(s[0]) + __uint_as_float(s[1]); }
__device__ __forceinline__ unsigned pack2(float a, float b) { bf16x2 t = {(__bf16)a, (__bf16)b}; return __builtin_bit_cast(unsigned, t); }
__device__ __forceinline__ float bflo(unsigned v) { return __uint_as_float(v << 16); }
__device__ __forceinline__ float bfhi(unsigned v) { return __uint_as_float(v & 0xffff0000u); }
__device__ __forceinline__ unsigned xor4(u32x4 v) { return (v.x ^ v.y) ^ (v.z ^ v.w); }
__device__ __forceinline__ unsigned add4(u32x4 v) { return (v.x + v.y) + (v.z + v.w); }
__device__ __forceinline__ float fsel(unsigned m, float a, float b) { return __uint_as_float((__float_as_uint(a) & m) | (__float_as_uint(b) & ~m)); }
__device__ __forceinline__ unsigned seed4(unsigned k0) { return (k0 ^ (k0 + PHI)) ^ ((k0 + 2u * PHI) ^ (k0 + 3u * PHI)); }
__device__ __forceinline__ f32x4 mfma(bf16x8 a, bf16x8 b, f32x4 c) { return __builtin_amdgcn_mfma_f32_16x16x32_bf16(a, b, c, 0, 0, 0); }
__device__ __forceinline__ bf16x8 as_frag(u32x4 v) { return __builtin_bit_cast(bf16x8, v); }
__device__ __forceinline__ u32x4 lds_ld16(LAS char* base, unsigned off) { return *(LAS u32x4*)(base + off); }
__device__ __forceinline__ bf16x8 tr_pair(LAS char* base, unsigned off0, unsigned off1) {
  s16x4 a = __builtin_amdgcn_ds_read_tr16_b64_v4i16((LAS s16x4*)(base + off0));
  s16x4 b = __builtin_amdgcn_ds_read_tr16_b64_v4i16((LAS s16x4*)(base + off1));
  return __builtin_bit_cast(bf16x8, __builtin_shufflevector(a, b, 0, 1, 2, 3, 4, 5, 6, 7));
}
#define BARRIER_LDS() asm volatile("s_waitcnt lgkmcnt(0)\n\ts_barrier" ::: "memory")
template <int N> __device__ __forceinline__ void wait_vm() { asm volatile("s_waitcnt vmcnt(%0)" ::"n"(N) : "memory"); }
#define FENCE() asm volatile("" ::: "memory")

__device__ __forceinline__ void fl_set(unsigned addr, unsigned v) { asm volatile("ds_write_b32 %0, %1" ::"v"(addr), "v"(v) : "memory"); }
__device__ __forceinline__ void fl_inc(unsigned addr, int lane) {
  if (lane == 0) asm volatile("ds_add_u32 %0, %1" ::"v"(addr), "v"(1u) : "memory");
}
__device__ __forceinline__ void fl_add(unsigned addr, int lane) {
  asm volatile("s_waitcnt lgkmcnt(0)" ::: "memory");
  fl_inc(addr, lane);
}
__device__ __forceinline__ unsigned fl_get(unsigned addr) {
  unsigned r; asm volatile("ds_read_b32 %0, %1\n\ts_waitcnt lgkmcnt(0)" : "=v"(r) : "v"(addr) : "memory");
  return __builtin_amdgcn_readfirstlane(r);
}
__device__ __forceinline__ void fl_wait(unsigned addr, unsigned want) {
  for (unsigned n = 0; fl_get(addr) < want && n < LSPIN_CAP; ++n) { }
}
__device__ __forceinline__ void fl_wait_sleep(unsigned addr, unsigned want) {
  for (unsigned n = 0; fl_get(addr) < want && n < LSPIN_CAP; ++n) __builtin_amdgcn_s_sleep(1);
}

__device__ __forceinline__ int slot_of(int seq) { return seq & 7; }
__device__ __forceinline__ int seq_v(int t) { return 16 + t + ((t >> 1) < 7 ? (t >> 1) : 7); }
__device__ __forceinline__ int seq_w(int g) { return 18 + 3 * g; }
__device__ __forceinline__ bool is_w(int k) { return k % 3 == 2 && k < 21; }
__device__ __forceinline__ int v_of(int k) { return k < 21 ? k - (k + 1) / 3 : k - 7; }
__device__ __forceinline__ void dma16(__amdgpu_buffer_rsrc_t r, LAS char* lds, unsigned voff, unsigned soff) {
  __builtin_amdgcn_raw_ptr_buffer_load_lds(r, (LAS void*)lds, 16, voff, soff, 0, AUX_NT);
}

__global__ void __launch_bounds__(512, 2) attn_block_kernel(Params p) {
  extern __shared__ __attribute__((aligned(16))) char smem_[];
  LAS char* smem = (LAS char*)smem_;
  const int tid = threadIdx.x, lane = tid & 63;
  const int wave = __builtin_amdgcn_readfirstlane(tid >> 6);
  const int wg = blockIdx.x, xc = wg & 7, ii = wg >> 3;
  const int sp = (xc & 1) | ((xc >> 2) << 1);
  const int kvh = ((xc >> 1) & 1) | ((ii & 3) << 1);
  const int b = ii >> 2;
  const int rg = b >> 1, e8 = (b & 1) * 4 + sp;
  const int cluster = b * 8 + kvh, j32 = kvh * 4 + sp;
  const int q16 = lane & 15, g4 = lane >> 4;
  ST(0);

  const __amdgpu_buffer_rsrc_t rws = __builtin_amdgcn_make_buffer_rsrc(p.ws, 0, W_END, 0x00020000);
  const size_t kvbase = ((size_t)cluster * SK + (SK - WIN)) * 512;

  unsigned wof[16][4];

  auto read_w = [&](int seq) {
    const unsigned qq = (lane & 15) >> 2, pq = lane & 3, r = 4 * g4 + qq;
    const unsigned base = L_RING + slot_of(seq) * TILE + r * 128 + (pq & 1) * 8;
#pragma unroll
    for (int u = 0; u < 4; ++u) {
      LAS char* a = smem + (base + (((2 * u + (pq >> 1)) ^ (((r >> 1) & 3) << 1)) << 4));
#pragma unroll
      for (int ht = 0; ht < 4; ++ht) {
        const u32x4 f = __builtin_bit_cast(u32x4, tr_pair(a, ht * 4096, ht * 4096 + 2048));
        wof[ht * 4 + u][0] = f.x; wof[ht * 4 + u][1] = f.y; wof[ht * 4 + u][2] = f.z; wof[ht * 4 + u][3] = f.w;
      }
    }
  };

  if (wave == 4) {
    fl_set(L_FL + lane * 4, 0u);
    fl_set(L_FL + 256 + lane * 4, 0u);
  }

  if (wave >= 6) {
    const __amdgpu_buffer_rsrc_t rk = __builtin_amdgcn_make_buffer_rsrc((void*)(p.kc + kvbase), 0, WIN * 512, 0x00020000);
    const __amdgpu_buffer_rsrc_t rv = __builtin_amdgcn_make_buffer_rsrc((void*)(p.vc + kvbase), 0, WIN * 512, 0x00020000);
    const __amdgpu_buffer_rsrc_t rwo = __builtin_amdgcn_make_buffer_rsrc((void*)p.wo, 0, 4096 * 896, 0x00020000);
    const unsigned worow = (unsigned)((2 * kvh) * 256 + 64 * sp);
    const unsigned hh = lane >> 5, pp = lane & 31;
    const int hf = wave - 6;
    auto issue = [&](int s) {
      LAS char* dst = smem + L_RING + slot_of(s) * TILE + hf * 8192;
      const int k = s - 16;
      if (s < 16) {
        const unsigned so = (unsigned)(s * 4 + sp) * TILE + hf * 8192;
#pragma unroll
        for (int i = 0; i < 8; ++i) dma16(rk, dst + i * 1024, hh * 512 + ((pp ^ (2 * i + hh)) << 4), so + i * 1024);
      } else if (is_w(k)) {
        const unsigned r8 = lane >> 3, p8 = lane & 7;
        const unsigned vo = r8 * 896 + (k / 3) * 128 + ((p8 ^ (((r8 >> 1) & 3) << 1)) << 4);
#pragma unroll
        for (int n = 0; n < 8; ++n) dma16(rwo, dst + n * 1024, vo, (worow + hf * 256 + n * 8) * 896);
      } else {
        const unsigned so = (unsigned)(v_of(k) * 4 + sp) * TILE + hf * 8192;
#pragma unroll
        for (int i = 0; i < 8; ++i) dma16(rv, dst + i * 1024, hh * 512 + ((pp ^ (((2 * i + hh) & 7) << 1)) << 4), so + i * 1024);
      }
    };
#pragma unroll
    for (int s = 0; s < LWIN - 1; ++s) issue(s);
    BARRIER_LDS();
    for (int s = LWIN - 1; s < NENT; ++s) {
      const int prev = s - 8;
      if (s >= 8) fl_wait_sleep(F_REL + prev * 4, (prev >= 16 && !is_w(prev - 16)) ? 4u : 1u);
      issue(s);
      wait_vm<8 * (LWIN - 1)>();
      fl_inc(F_LAND + (s - (LWIN - 1)) * 4, lane);
      ST(12 + ((s - (LWIN - 1)) >> 1));
    }
    ST(1);
    static_assert(LWIN == 5, "the drain below announces the last LWIN - 1 entries");
    wait_vm<24>(); fl_inc(F_LAND + (NENT - 4) * 4, lane);
    wait_vm<16>(); fl_inc(F_LAND + (NENT - 3) * 4, lane);
    wait_vm<8>(); fl_inc(F_LAND + (NENT - 2) * 4, lane);
    wait_vm<0>(); fl_inc(F_LAND + (NENT - 1) * 4, lane);
    if (wave == 6) { fl_wait(F_LAND + seq_w(6) * 4, 2u); read_w(seq_w(6)); }
    else {
#pragma unroll
      for (int i = 0; i < 16; ++i) { wof[i][0] = 0u; wof[i][1] = 0u; wof[i][2] = 0u; wof[i][3] = 0u; }
    }
    ST(2);
  } else {
    u32x4 wraw[2][8], xf[2][2];
    u32x2 ep = {0u, 0u};
    if (wave < 4) {
      const __amdgpu_buffer_rsrc_t rx = __builtin_amdgcn_make_buffer_rsrc((void*)(p.x + rg * 16 * 896), 0, 16 * 896, 0x00020000);
      const __amdgpu_buffer_rsrc_t rq = __builtin_amdgcn_make_buffer_rsrc((void*)(p.wq + kvh * 1024 + e8 * 128), 0, 448 * 8192 - (kvh * 1024 + e8 * 128), 0x00020000);
      const unsigned vx = q16 * 896 + g4 * 16, vq = (32 * ((lane >> 3) & 1) + 8 * g4) * 8192 + (lane & 7) * 16;
#pragma unroll
      for (int mm = 0; mm < 2; ++mm) {
        const unsigned m = 2 * wave + mm;
        xf[mm][0] = __builtin_amdgcn_raw_buffer_load_b128(rx, m < 7 ? vx : OOB, m < 7 ? m * 128 : 0, 0);
        xf[mm][1] = __builtin_amdgcn_raw_buffer_load_b128(rx, m < 7 ? vx : OOB, m < 7 ? m * 128 + 64 : 0, 0);
      }
      ep = __builtin_amdgcn_raw_buffer_load_b64(rws, tid == 0 ? W_E + rg * 256 : OOB, 0, AUX_SC1);
#pragma unroll
      for (int mm = 0; mm < 2; ++mm) {
        const unsigned m = 2 * wave + mm;
#pragma unroll
        for (int j = 0; j < 8; ++j) wraw[mm][j] = __builtin_amdgcn_raw_buffer_load_b128(rq, m < 7 ? vq + j * 8192 : OOB, m < 7 ? m * 64 * 8192 : 0, AUX_NT);
      }
    }
    BARRIER_LDS();
    f32x4 sc[3][2];
    bf16x8 qf[8];
    auto load_q = [&]() {
#pragma unroll
      for (int ts = 0; ts < 8; ++ts) qf[ts] = as_frag(lds_ld16(smem, L_QIMG + q16 * 512 + (((4 * ts + g4) ^ q16) << 4)));
    };
    auto park_k = [&](int t, bf16x8 (&kf)[16]) {
      fl_wait(F_LAND + t * 4, 2u);
      const unsigned so = L_RING + slot_of(t) * TILE + q16 * 512;
#pragma unroll
      for (int i = 0; i < 4; ++i) {
        LAS char* kp = smem + (so + (((4 * i + g4) ^ q16) << 4));
        kf[i] = as_frag(lds_ld16(kp, 0)); kf[8 + i] = as_frag(lds_ld16(kp, 8192));
        kf[4 + i] = as_frag(lds_ld16(kp, 256)); kf[12 + i] = as_frag(lds_ld16(kp, 8192 + 256));
      }
      fl_add(F_REL + t * 4, lane);
    };
    auto qk = [&](const bf16x8 (&kf)[16], f32x4 (&s)[2]) {
      f32x4 s0 = {0.f, 0.f, 0.f, 0.f}, s1 = {0.f, 0.f, 0.f, 0.f};
#pragma unroll
      for (int i = 0; i < 8; ++i) { s0 = mfma(kf[i], qf[i], s0); s1 = mfma(kf[8 + i], qf[i], s1); }
      s[0] = s0; s[1] = s1;
    };
    if (wave < 4) {
      {
        f32x4 a0[8], a1[8];
#pragma unroll
        for (int u = 0; u < 8; ++u) { a0[u] = f32x4{0.f, 0.f, 0.f, 0.f}; a1[u] = f32x4{0.f, 0.f, 0.f, 0.f}; }
#pragma unroll
        for (int mm = 0; mm < 2; ++mm)
#pragma unroll
          for (int u = 0; u < 8; ++u) {
            u32x4 bq;
#pragma unroll
            for (int pp = 0; pp < 4; ++pp)
              bq[pp] = __builtin_amdgcn_perm(wraw[mm][2 * pp + 1][u >> 1], wraw[mm][2 * pp][u >> 1], (u & 1) ? 0x07060302u : 0x05040100u);
            a0[u] = mfma(as_frag(xf[mm][0]), as_frag(bq), a0[u]);
            a1[u] = mfma(as_frag(xf[mm][1]), as_frag(bq), a1[u]);
          }
        const bool hx = (lane & 8) != 0;
        const unsigned mx8 = hx ? 0xffffffffu : 0u;
        float r[8][4];
#pragma unroll
        for (int u = 0; u < 8; ++u)
#pragma unroll
          for (int i = 0; i < 4; ++i) { const float own = fsel(mx8, a1[u][i], a0[u][i]); r[u][i] = own + __uint_as_float(dpp<0x128>(__float_as_uint(own))); }
#pragma unroll
        for (int k = 0; k < 2; ++k) {
          LAS char* d = smem + (L_QP + wave * 4096 + (4 * g4 + (hx ? 2 : 0) + k) * 256 + (lane & 7) * 32);
          f32x4 lo, hi;
#pragma unroll
          for (int u = 0; u < 4; ++u) { lo[u] = fsel(mx8, r[u][2 + k], r[u][k]); hi[u] = fsel(mx8, r[4 + u][2 + k], r[4 + u][k]); }
          *(LAS f32x4*)d = lo; *(LAS f32x4*)(d + 16) = hi;
        }
        if (tid == 0) {
          const unsigned tlo = ep.x + 1u, thi = ep.y + ((tlo == 0u) ? 1u : 0u);
          *(LAS u32x4*)(smem + L_T) = u32x4{tlo, thi, tlo, (tlo * 0x9E3779B1u) ^ thi};
        }
      }
      fl_add(C_QSTG, lane);
      ST(1);
      if (wave < 2) {
        fl_wait(C_QSTG, 4u);
        const u32x4 tw = *(LAS u32x4*)(smem + L_T);
        const unsigned seedA = __builtin_amdgcn_readfirstlane(tw.z), seedB = __builtin_amdgcn_readfirstlane(tw.w);
        LAS char* d = smem + (L_QP + (8 * wave + (lane >> 3)) * 256 + (lane & 7) * 32);
        f32x4 lo = *(LAS f32x4*)d, hi = *(LAS f32x4*)(d + 16);
#pragma unroll
        for (int w = 1; w < 4; ++w) { lo += *(LAS f32x4*)(d + w * 4096); hi += *(LAS f32x4*)(d + w * 4096 + 16); }
        const u32x4 v = {pack2(lo[0], lo[1]), pack2(lo[2], lo[3]), pack2(hi[0], hi[1]), pack2(hi[2], hi[3])};
        const unsigned piece = ((2 * rg + wave) * 8 + kvh) * 8 + e8;
        __builtin_amdgcn_raw_buffer_store_b128(v, rws, W_Q + piece * 1024 + lane * 16, 0, AUX_ST);
        const unsigned a = wave_sum(add4(v)) + seedA;
        const unsigned x = wave_xor(xor4(v)) ^ (seedB + e8 * PHI);
        __builtin_amdgcn_raw_buffer_store_b128(u32x4{a, x, seedA, 0u}, rws, lane == 0 ? W_QC + piece * 16 : OOB, 0, AUX_ST);
      }
      ST(2);
      bf16x8 k0[16], k1[16];
      park_k(4 + wave, k0);
      park_k(8 + wave, k1);
      ST(3);
      fl_wait(C_QIMG, 2u);
      load_q();
      qk(k0, sc[0]);
      qk(k1, sc[1]);
      park_k(12 + wave, k0);
      qk(k0, sc[2]);
    } else {
      bf16x8 ka[16], kb[16];
      park_k(wave - 4, ka);
      park_k(wave - 2, kb);
      ST(2);
      fl_wait_sleep(C_QSTG, 4u);
      const u32x4 tw = *(LAS u32x4*)(smem + L_T);
      const unsigned seedA = __builtin_amdgcn_readfirstlane(tw.z), seedB = __builtin_amdgcn_readfirstlane(tw.w);
      const unsigned p0 = 4 * (wave - 4), pbase = (b * 8 + kvh) * 8 + p0, sxq = seed4(seedB + p0 * PHI);
      u32x4 v[4];
      for (unsigned spins = 0;; ++spins) {
        FENCE();
#pragma unroll
        for (int k = 0; k < 4; ++k) v[k] = __builtin_amdgcn_raw_buffer_load_b128(rws, W_Q + (pbase + k) * 1024 + lane * 16, 0, AUX_SC1);
        const u32x2 c = __builtin_amdgcn_raw_buffer_load_b64(rws, lane < 4 ? W_QC + (pbase + lane) * 16 : OOB, 0, AUX_SC1);
        const unsigned a = wave_sum((add4(v[0]) + add4(v[1])) + (add4(v[2]) + add4(v[3])) - c.x);
        const unsigned x = wave_xor((xor4(v[0]) ^ xor4(v[1])) ^ (xor4(v[2]) ^ xor4(v[3])) ^ c.y);
        const bool ok = (a == 0u - 4u * seedA) && (x == sxq);
        if (__builtin_amdgcn_readfirstlane(ok) || spins > SPIN_CAP) break;
        __builtin_amdgcn_s_sleep(1);
      }
#pragma unroll
      for (int k = 0; k < 4; ++k) {
        const unsigned row = (wave - 4) * 8 + (lane >> 3), c16 = k * 8 + (lane & 7);
        *(LAS u32x4*)(smem + L_QIMG + row * 512 + ((c16 ^ row) << 4)) = v[k];
      }
      fl_add(C_QIMG, lane);
      ST(3);
      fl_wait(C_QIMG, 2u);
      load_q();
      qk(ka, sc[0]);
      qk(kb, sc[1]);
      sc[2][0] = f32x4{-INFINITY, -INFINITY, -INFINITY, -INFINITY}; sc[2][1] = sc[2][0];
    }
    ST(4);
    float mx = -INFINITY;
#pragma unroll
    for (int i = 0; i < 3; ++i)
#pragma unroll
      for (int k = 0; k < 2; ++k) mx = fmaxf(mx, fmaxf(fmaxf(sc[i][k][0], sc[i][k][1]), fmaxf(sc[i][k][2], sc[i][k][3])));
    mx = fmax32(fmax16(mx));
    if (lane < 16) *(LAS float*)(smem + L_MX + wave * 64 + lane * 4) = mx;
    fl_add(C_MX, lane);
    fl_wait(C_MX, 6u);
    {
      LAS float* pm = (LAS float*)(smem + L_MX + q16 * 4);
      mx = fmaxf(fmaxf(fmaxf(pm[0], pm[16]), fmaxf(pm[32], pm[48])), fmaxf(pm[64], pm[80]));
    }
    const float mc = mx * CSC;
    float ls = 0.f;
#pragma unroll
    for (int i = 0; i < 3; ++i)
#pragma unroll
      for (int k = 0; k < 2; ++k)
#pragma unroll
        for (int e = 0; e < 4; ++e) { const float pe = __builtin_amdgcn_exp2f(sc[i][k][e] * CSC - mc); sc[i][k][e] = pe; ls += pe; }
    ls = fadd32(fadd16(ls));
    if (lane < 16) *(LAS float*)(smem + L_LS + wave * 64 + lane * 4) = ls;
    fl_add(C_LS, lane);
    fl_wait(C_LS, 6u);
    {
      LAS float* pl = (LAS float*)(smem + L_LS + q16 * 4);
      ls = ((pl[0] + pl[16]) + (pl[32] + pl[48])) + (pl[64] + pl[80]);
    }
    const float inv = 1.0f / ls;
#pragma unroll
    for (int i = 0; i < 3; ++i) {
      const int t = wave < 4 ? 4 + 4 * i + wave : (wave - 4) + 2 * i;
      if (wave < 4 || i < 2)
        *(LAS u32x4*)(smem + L_P + t * 1024 + lane * 16) =
            u32x4{pack2(sc[i][0][0] * inv, sc[i][0][1] * inv), pack2(sc[i][0][2] * inv, sc[i][0][3] * inv),
                  pack2(sc[i][1][0] * inv, sc[i][1][1] * inv), pack2(sc[i][1][2] * inv, sc[i][1][3] * inv)};
    }
    fl_add(C_P, lane);
    const u32x4 tw = *(LAS u32x4*)(smem + L_T);
    const unsigned seedA = __builtin_amdgcn_readfirstlane(tw.z), seedB = __builtin_amdgcn_readfirstlane(tw.w);
    if (wave == 4) {
      const unsigned lamb = __float_as_uint(mc + __builtin_amdgcn_logf(ls));
      const unsigned a = row_sum(lamb) + seedA, x = row_xor(lamb) ^ (seedB + sp * PHI);
      const unsigned hb = W_LAM + (cluster * 4 + sp) * 128;
      __builtin_amdgcn_raw_buffer_store_b32(lamb, rws, lane < 16 ? hb + lane * 4 : OOB, 0, AUX_ST);
      __builtin_amdgcn_raw_buffer_store_b64(u32x2{a, x}, rws, lane == 0 ? hb + 64 : OOB, 0, AUX_ST);
    }
    ST(5);
    if (wave < 4) {
      unsigned vad[4];
#pragma unroll
      for (int i = 0; i < 4; ++i) {
        const unsigned qq = (lane & 15) >> 2, pq = lane & 3, r = 4 * g4 + qq;
        vad[i] = L_RING + r * 512 + (((8 * wave + 2 * i + (pq >> 1)) ^ ((r & 7) << 1)) << 4) + (pq & 1) * 8;
      }
      f32x4 o[4];
#pragma unroll
      for (int j = 0; j < 4; ++j) o[j] = f32x4{0.f, 0.f, 0.f, 0.f};
      fl_wait(C_P, 6u);
      for (int t = 0; t < 16; ++t) {
        const int sq = seq_v(t);
        const u32x4 pf = lds_ld16(smem, L_P + t * 1024 + lane * 16);
        fl_wait(F_LAND + sq * 4, 2u);
        const unsigned so = slot_of(sq) * TILE;
        bf16x8 vf[4];
#pragma unroll
        for (int j = 0; j < 4; ++j) vf[j] = tr_pair(smem + (vad[j] + so), 0, 8192);
        fl_add(F_REL + sq * 4, lane);
#pragma unroll
        for (int j = 0; j < 4; ++j) o[j] = mfma(vf[j], as_frag(pf), o[j]);
        if (t == 2 * wave + 2) { fl_wait(F_LAND + seq_w(wave) * 4, 2u); read_w(seq_w(wave)); fl_add(F_REL + seq_w(wave) * 4, lane); }
      }
      ST(6);
      const unsigned ub = W_H1 + ((cluster * 4 + wave) * 4 + sp) * H1_UNIT;
      const u32x4 v0 = {pack2(o[0][0], o[0][1]), pack2(o[0][2], o[0][3]), pack2(o[1][0], o[1][1]), pack2(o[1][2], o[1][3])};
      const u32x4 v1 = {pack2(o[2][0], o[2][1]), pack2(o[2][2], o[2][3]), pack2(o[3][0], o[3][1]), pack2(o[3][2], o[3][3])};
      __builtin_amdgcn_raw_buffer_store_b128(v0, rws, ub + lane * 16, 0, AUX_ST);
      __builtin_amdgcn_raw_buffer_store_b128(v1, rws, ub + 1024 + lane * 16, 0, AUX_ST);
      const unsigned a = oct_sum(add4(v0) + add4(v1)) + seedA;
      const unsigned x = oct_xor(xor4(v0) ^ xor4(v1)) ^ (seedB + sp * PHI);
      __builtin_amdgcn_raw_buffer_store_b128(u32x4{a, x, seedA, 0u}, rws, (lane & 7) == 0 ? ub + 2048 + (lane >> 3) * 16 : OOB, 0, AUX_ST);
    } else {
      fl_wait_sleep(F_LAND + seq_w(wave) * 4, 2u);
      read_w(seq_w(wave));
      fl_add(F_REL + seq_w(wave) * 4, lane);
      if (wave == 5) {
        const unsigned hb = W_LAM + cluster * 4 * 128, sx4 = seed4(seedB);
        unsigned lam;
        for (unsigned spins = 0;; ++spins) {
          FENCE();
          lam = __builtin_amdgcn_raw_buffer_load_b32(rws, hb + g4 * 128 + q16 * 4, 0, AUX_SC1);
          const u32x2 c = __builtin_amdgcn_raw_buffer_load_b64(rws, lane < 4 ? hb + lane * 128 + 64 : OOB, 0, AUX_SC1);
          const bool ok = (wave_sum(lam - c.x) == 0u - 4u * seedA) && (wave_xor(lam ^ c.y) == sx4);
          if (__builtin_amdgcn_readfirstlane(ok) || spins > SPIN_CAP) break;
          __builtin_amdgcn_s_sleep(8);
        }
        const float l = __uint_as_float(lam);
        const float w = __builtin_amdgcn_exp2f(l - fmax32(fmax16(l)));
        *(LAS float*)(smem + L_WGT + q16 * 16 + g4 * 4) = w / fadd32(fadd16(w));
        fl_add(C_WGT, lane);
      }
    }
  }
  ST(7);

  const u32x4 tw = *(LAS u32x4*)(smem + L_T);
  const unsigned seedA = __builtin_amdgcn_readfirstlane(tw.z), seedB = __builtin_amdgcn_readfirstlane(tw.w);
  {
    const int gg = wave >> 1, hf = wave & 1;
    const int tt = lane >> 5, s8 = (lane >> 2) & 7, c4 = lane & 3;
    fl_wait(C_WGT, 1u);
    const f32x4 wv = *(LAS f32x4*)(smem + L_WGT + (hf * 8 + s8) * 16);
    ST(8);
    const unsigned ub = W_H1 + (cluster * 4 + sp) * 4 * H1_UNIT, sx4 = seed4(seedB);
    const unsigned po = tt * 1024 + (gg * 16 + hf * 8 + s8) * 16 + c4 * 4;
    const unsigned ca = lane < 4 ? ub + lane * H1_UNIT + 2048 + (gg * 2 + hf) * 16 : OOB;
    unsigned pay[4] = {0u, 0u, 0u, 0u};
    unsigned need = 0xFu, seen = 0u;
    for (unsigned spins = 0;; ++spins) {
      FENCE();
      const u32x4 c = __builtin_amdgcn_raw_buffer_load_b128(rws, ca, 0, AUX_SC1);
      unsigned np[4];
#pragma unroll
      for (int s = 0; s < 4; ++s) np[s] = __builtin_amdgcn_raw_buffer_load_b32(rws, (need >> s) & 1u ? ub + s * H1_UNIT + po : OOB, 0, AUX_SC1);
#pragma unroll
      for (int s = 0; s < 4; ++s) pay[s] = (need >> s) & 1u ? np[s] : pay[s];
      const unsigned now = (unsigned)__ballot(c.z == seedA) & 0xFu;
      if (now == 0xFu) {
        const unsigned a = wave_sum((pay[0] + pay[1]) + (pay[2] + pay[3]) - c.x);
        const unsigned x = wave_xor((pay[0] ^ pay[1]) ^ (pay[2] ^ pay[3]) ^ c.y);
        const bool ok = (a == 0u - 4u * seedA) && (x == sx4);
        if (__builtin_amdgcn_readfirstlane(ok) || spins > SPIN_CAP) break;
        need = 0xFu;
      } else {
        if (spins > SPIN_CAP) break;
        need = ~(now & seen) & 0xFu;
        __builtin_amdgcn_s_sleep(2);
      }
      seen = now;
    }
    const float olo = (wv[0] * bflo(pay[0]) + wv[1] * bflo(pay[1])) + (wv[2] * bflo(pay[2]) + wv[3] * bflo(pay[3]));
    const float ohi = (wv[0] * bfhi(pay[0]) + wv[1] * bfhi(pay[1])) + (wv[2] * bfhi(pay[2]) + wv[3] * bfhi(pay[3]));
    const unsigned ov = pack2(olo, ohi);
    LAS unsigned* dst = (LAS unsigned*)(smem + L_OFR + hf * 2048 + tt * 1024 + (gg * 16 + s8) * 16 + c4 * 4);
    dst[0] = ov; dst[32] = ov;
  }
  ST(9);
  BARRIER_LDS();
  const unsigned sb = W_H2 + (b * 32 + j32) * H2_UNIT;
  if (wave < 7) {
    bf16x8 of[4];
#pragma unroll
    for (int ht = 0; ht < 4; ++ht) of[ht] = as_frag(lds_ld16(smem + (L_OFR + lane * 16), ht * 1024));
    __builtin_amdgcn_sched_barrier(0);
    f32x4 c[4];
#pragma unroll
    for (int u = 0; u < 4; ++u) c[u] = f32x4{0.f, 0.f, 0.f, 0.f};
#pragma unroll
    for (int ht = 0; ht < 4; ++ht)
#pragma unroll
      for (int u = 0; u < 4; ++u) c[u] = mfma(as_frag(u32x4{wof[ht * 4 + u][0], wof[ht * 4 + u][1], wof[ht * 4 + u][2], wof[ht * 4 + u][3]}), of[ht], c[u]);
    const unsigned msk = q16 >= 8 ? 0xffffffffu : 0u;
    const u32x4 lo4 = {pack2(c[0][0], c[0][1]), pack2(c[0][2], c[0][3]), pack2(c[1][0], c[1][1]), pack2(c[1][2], c[1][3])};
    const u32x4 hi4 = {pack2(c[2][0], c[2][1]), pack2(c[2][2], c[2][3]), pack2(c[3][0], c[3][1]), pack2(c[3][2], c[3][3])};
    const u32x4 v = (hi4 & msk) | (lo4 & ~msk);
    __builtin_amdgcn_raw_buffer_store_b128(v, rws, sb + wave * 1024 + lane * 16, 0, AUX_ST);
    const unsigned a = row_sum(add4(v)) + seedA;
    const unsigned x = row_xor(xor4(v)) ^ (seedB + j32 * PHI);
    __builtin_amdgcn_raw_buffer_store_b128(u32x4{a, x, seedA, 0u}, rws, q16 == 0 ? sb + 7168 + (wave * 4 + g4) * 16 : OOB, 0, AUX_ST);
  }
  ST(10);
  if (j32 < 28) {
    const int G = j32 >> 2, g = j32 & 3;
    const unsigned bb = W_H2 + b * 32 * H2_UNIT;
    const unsigned po = bb + (4 * wave + g4) * H2_UNIT + G * 1024 + (g * 16 + q16) * 16;
    const unsigned sx = seed4(seedB + (4 * wave) * PHI);
    const unsigned ca = lane < 4 ? bb + (4 * wave + lane) * H2_UNIT + 7168 + j32 * 16 : OOB;
    for (unsigned spins = 0; spins < SPIN_CAP; ++spins) {
      FENCE();
      const unsigned cx = __builtin_amdgcn_raw_buffer_load_b32(rws, ca + 8, 0, AUX_SC1);
      if (!__any(lane < 4 && cx != seedA)) break;
      __builtin_amdgcn_s_sleep(2);
    }
    u32x4 v;
    for (unsigned spins = 0;; ++spins) {
      FENCE();
      const u32x4 c = __builtin_amdgcn_raw_buffer_load_b128(rws, ca, 0, AUX_SC1);
      v = __builtin_amdgcn_raw_buffer_load_b128(rws, po, 0, AUX_SC1);
      const unsigned a = wave_sum(add4(v) - c.x);
      const unsigned x = wave_xor(xor4(v) ^ c.y);
      const bool ok = (a == 0u - 4u * seedA) && (x == sx);
      if (__builtin_amdgcn_readfirstlane(ok) || spins > SPIN_CAP) break;
      __builtin_amdgcn_s_sleep(1);
    }
    f32x4 s0 = {bflo(v.x), bfhi(v.x), bflo(v.y), bfhi(v.y)}, s1 = {bflo(v.z), bfhi(v.z), bflo(v.w), bfhi(v.w)};
#pragma unroll
    for (int u = 0; u < 4; ++u) { s0[u] = fadd32(fadd16(s0[u])); s1[u] = fadd32(fadd16(s1[u])); }
    if (lane < 16) { *(LAS f32x4*)(smem + L_RED + wave * 512 + lane * 32) = s0; *(LAS f32x4*)(smem + L_RED + wave * 512 + lane * 32 + 16) = s1; }
    ST(11);
    BARRIER_LDS();
    if (wave == 0 && lane < 32) {
      LAS char* rp = smem + (L_RED + q16 * 32 + (lane >> 4) * 16);
      f32x4 t = *(LAS f32x4*)rp;
#pragma unroll
      for (int w = 1; w < 8; ++w) t += *(LAS f32x4*)(rp + w * 512);
      const int col = 64 * G + 16 * (2 * (q16 >> 3) + (lane >> 4)) + 4 * g;
      *(GAS u32x2*)(p.out + ((size_t)(b * 8 + (q16 & 7)) * DM + col) * 2) = u32x2{pack2(t[0], t[1]), pack2(t[2], t[3])};
      if (j32 == 0 && (b & 1) == 0 && lane == 0)
        __hip_atomic_store((GAS u64*)(p.ws + W_E + rg * 256), ((u64)tw.y << 32) | tw.x, __ATOMIC_RELAXED, __HIP_MEMORY_SCOPE_AGENT);
    }
  }
  ST(31);
}

}

extern "C" void kernel_launch(void* const* d_in, const int* in_sizes, int n_in, void* d_out, int out_size, void* d_ws,
                              size_t ws_size, hipStream_t stream) {
  Params p;
  p.x = (const char*)d_in[0];
  p.kc = (const char*)d_in[1];
  p.vc = (const char*)d_in[2];
  p.wq = (const char*)d_in[3];
  p.wo = (const char*)d_in[4];
  p.out = (char*)d_out;
  p.ws = (char*)d_ws;
  (void)hipFuncSetAttribute((const void*)attn_block_kernel, hipFuncAttributeMaxDynamicSharedMemorySize, L_TOTAL);
  hipLaunchKernelGGL(attn_block_kernel, dim3(256), dim3(512), L_TOTAL, stream, p);
}
```

```cpp
#include <hip/hip_runtime.h>
#include <stdint.h>

typedef unsigned long long u64;
typedef unsigned u32x4 __attribute__((ext_vector_type(4)));
typedef unsigned u32x2 __attribute__((ext_vector_type(2)));
typedef float f32x4 __attribute__((ext_vector_type(4)));
typedef __bf16 bf16x8 __attribute__((ext_vector_type(8)));
typedef __bf16 bf16x2 __attribute__((ext_vector_type(2)));
typedef short s16x4 __attribute__((ext_vector_type(4)));
#define LAS __attribute__((address_space(3)))
#define GAS __attribute__((address_space(1)))

namespace {

constexpr int SK = 8192, WIN = 2048, DM = 448;
constexpr int TILE = 16384;
constexpr int NENT = 39;
constexpr int LWIN = 5;
constexpr int PACE = 55, PACE_OFF = 80;
constexpr int L_RING = 0;
constexpr int L_P = 131072;
constexpr int L_QP = L_P;
constexpr int L_QIMG = 147456;
constexpr int L_OFR = L_QIMG;
constexpr int L_RED = L_QIMG + 4096;
constexpr int L_WGT = 155648;
constexpr int L_MX = 157696;
constexpr int L_LS = 158208;
constexpr int L_FL = 158720;
constexpr int F_LAND = L_FL, F_REL = L_FL + 160, C_QSTG = L_FL + 324, C_QIMG = L_FL + 328, C_MX = L_FL + 332,
              C_LS = L_FL + 336, C_P = L_FL + 340, C_WGT = L_FL + 344;
constexpr int L_T = L_FL + 512;
constexpr int L_TOTAL = L_T + 16;
constexpr unsigned W_E = 0;
constexpr unsigned W_Q = 4096;
constexpr unsigned W_QC = W_Q + 512 * 1024;
constexpr unsigned W_LAM = W_QC + 8192;
constexpr unsigned W_H1 = W_LAM + 256 * 128;
constexpr unsigned H1_UNIT = 2176;
constexpr unsigned W_H2 = W_H1 + 64 * 16 * H1_UNIT;
constexpr unsigned H2_UNIT = 7680;
constexpr unsigned W_END = W_H2 + 256 * H2_UNIT;
constexpr unsigned OOB = 0x7fffffe0u;
constexpr unsigned SPIN_CAP = 1u << 16, LSPIN_CAP = 1u << 22;
constexpr unsigned PHI = 0x9E3779B9u;
constexpr int AUX_NT = 2, AUX_SC1 = 16;
constexpr float CSC = 0.0625f * 1.4426950408889634f;

struct Params {
  const char* x; const char* kc; const char* vc; const char* wq; const char* wo; char* out; char* ws;
};

template <int CTRL> __device__ __forceinline__ unsigned dpp(unsigned v) { return __builtin_amdgcn_update_dpp(0u, v, CTRL, 0xf, 0xf, true); }
__device__ __forceinline__ unsigned row_sum(unsigned v) { v += dpp<0x128>(v); v += dpp<0x124>(v); v += dpp<0x122>(v); v += dpp<0x121>(v); return v; }
__device__ __forceinline__ unsigned row_xor(unsigned v) { v ^= dpp<0x128>(v); v ^= dpp<0x124>(v); v ^= dpp<0x122>(v); v ^= dpp<0x121>(v); return v; }
__device__ __forceinline__ unsigned oct_sum(unsigned v) { v += dpp<0xB1>(v); v += dpp<0x4E>(v); v += dpp<0x141>(v); return v; }
__device__ __forceinline__ unsigned oct_xor(unsigned v) { v ^= dpp<0xB1>(v); v ^= dpp<0x4E>(v); v ^= dpp<0x141>(v); return v; }
__device__ __forceinline__ unsigned sum16(unsigned v) { auto s = __builtin_amdgcn_permlane16_swap(v, v, false, false); return s[0] + s[1]; }
__device__ __forceinline__ unsigned sum32(unsigned v) { auto s = __builtin_amdgcn_permlane32_swap(v, v, false, false); return s[0] + s[1]; }
__device__ __forceinline__ unsigned xor16(unsigned v) { auto s = __builtin_amdgcn_permlane16_swap(v, v, false, false); return s[0] ^ s[1]; }
__device__ __forceinline__ unsigned xor32(unsigned v) { auto s = __builtin_amdgcn_permlane32_swap(v, v, false, false); return s[0] ^ s[1]; }
__device__ __forceinline__ unsigned wave_sum(unsigned v) { return sum32(sum16(row_sum(v))); }
__device__ __forceinline__ unsigned wave_xor(unsigned v) { return xor32(xor16(row_xor(v))); }
__device__ __forceinline__ float fmax16(float v) { auto s = __builtin_amdgcn_permlane16_swap(__float_as_uint(v), __float_as_uint(v), false, false); return fmaxf(__uint_as_float(s[0]), __uint_as_float(s[1])); }
__device__ __forceinline__ float fmax32(float v) { auto s = __builtin_amdgcn_permlane32_swap(__float_as_uint(v), __float_as_uint(v), false, false); return fmaxf(__uint_as_float(s[0]), __uint_as_float(s[1])); }
__device__ __forceinline__ float fadd16(float v) { auto s = __builtin_amdgcn_permlane16_swap(__float_as_uint(v), __float_as_uint(v), false, false); return __uint_as_float(s[0]) + __uint_as_float(s[1]); }
__device__ __forceinline__ float fadd32(float v) { auto s = __builtin_amdgcn_permlane32_swap(__float_as_uint(v), __float_as_uint(v), false, false); return __uint_as_float(s[0]) + __uint_as_float(s[1]); }
__device__ __forceinline__ unsigned pack2(float a, float b) { bf16x2 t = {(__bf16)a, (__bf16)b}; return __builtin_bit_cast(unsigned, t); }
__device__ __forceinline__ float bflo(unsigned v) { return __uint_as_float(v << 16); }
__device__ __forceinline__ float bfhi(unsigned v) { return __uint_as_float(v & 0xffff0000u); }
__device__ __forceinline__ unsigned xor4(u32x4 v) { return (v.x ^ v.y) ^ (v.z ^ v.w); }
__device__ __forceinline__ unsigned add4(u32x4 v) { return (v.x + v.y) + (v.z + v.w); }
__device__ __forceinline__ float fsel(unsigned m, float a, float b) { return __uint_as_float((__float_as_uint(a) & m) | (__float_as_uint(b) & ~m)); }
__device__ __forceinline__ unsigned seed4(unsigned k0) { return (k0 ^ (k0 + PHI)) ^ ((k0 + 2u * PHI) ^ (k0 + 3u * PHI)); }
__device__ __forceinline__ f32x4 mfma(bf16x8 a, bf16x8 b, f32x4 c) { return __builtin_amdgcn_mfma_f32_16x16x32_bf16(a, b, c, 0, 0, 0); }
__device__ __forceinline__ bf16x8 as_frag(u32x4 v) { return __builtin_bit_cast(bf16x8, v); }
__device__ __forceinline__ u32x4 lds_ld16(LAS char* base, unsigned off) { return *(LAS u32x4*)(base + off); }
__device__ __forceinline__ bf16x8 tr_pair(LAS char* base, unsigned off0, unsigned off1) {
  s16x4 a = __builtin_amdgcn_ds_read_tr16_b64_v4i16((LAS s16x4*)(base + off0));
  s16x4 b = __builtin_amdgcn_ds_read_tr16_b64_v4i16((LAS s16x4*)(base + off1));
  return __builtin_bit_cast(bf16x8, __builtin_shufflevector(a, b, 0, 1, 2, 3, 4, 5, 6, 7));
}
#define BARRIER_LDS() asm volatile("s_waitcnt lgkmcnt(0)\n\ts_barrier" ::: "memory")
template <int N> __device__ __forceinline__ void wait_vm() { asm volatile("s_waitcnt vmcnt(%0)" ::"n"(N) : "memory"); }
#define FENCE() asm volatile("" ::: "memory")

__device__ __forceinline__ void fl_set(unsigned addr, unsigned v) { asm volatile("ds_write_b32 %0, %1" ::"v"(addr), "v"(v) : "memory"); }
__device__ __forceinline__ void fl_inc(unsigned addr, int lane) {
  if (lane == 0) asm volatile("ds_add_u32 %0, %1" ::"v"(addr), "v"(1u) : "memory");
}
__device__ __forceinline__ void fl_add(unsigned addr, int lane) {
  asm volatile("s_waitcnt lgkmcnt(0)" ::: "memory");
  fl_inc(addr, lane);
}
__device__ __forceinline__ unsigned fl_get(unsigned addr) {
  unsigned r; asm volatile("ds_read_b32 %0, %1\n\ts_waitcnt lgkmcnt(0)" : "=v"(r) : "v"(addr) : "memory");
  return __builtin_amdgcn_readfirstlane(r);
}
__device__ __forceinline__ void fl_wait(unsigned addr, unsigned want) {
  for (unsigned n = 0; fl_get(addr) < want && n < LSPIN_CAP; ++n) { }
}
__device__ __forceinline__ void fl_wait_sleep(unsigned addr, unsigned want) {
  for (unsigned n = 0; fl_get(addr) < want && n < LSPIN_CAP; ++n) __builtin_amdgcn_s_sleep(1);
}

__device__ __forceinline__ int slot_of(int seq) { return seq & 7; }
__device__ __forceinline__ int seq_v(int t) { return 16 + t + ((t >> 1) < 7 ? (t >> 1) : 7); }
__device__ __forceinline__ int seq_w(int g) { return 18 + 3 * g; }
__device__ __forceinline__ bool is_w(int k) { return k % 3 == 2 && k < 21; }
__device__ __forceinline__ int v_of(int k) { return k < 21 ? k - (k + 1) / 3 : k - 7; }
__device__ __forceinline__ void dma16(__amdgpu_buffer_rsrc_t r, LAS char* lds, unsigned voff, unsigned soff) {
  __builtin_amdgcn_raw_ptr_buffer_load_lds(r, (LAS void*)lds, 16, voff, soff, 0, AUX_NT);
}

__global__ void __launch_bounds__(512, 2) attn_block_kernel(Params p) {
  extern __shared__ __attribute__((aligned(16))) char smem_[];
  LAS char* smem = (LAS char*)smem_;
  const int tid = threadIdx.x, lane = tid & 63;
  const int wave = __builtin_amdgcn_readfirstlane(tid >> 6);
  const int wg = blockIdx.x, xc = wg & 7, ii = wg >> 3;
  const int sp = (xc & 1) | ((xc >> 2) << 1);
  const int kvh = ((xc >> 1) & 1) | ((ii & 3) << 1);
  const int b = ii >> 2;
  const int rg = b >> 1, e8 = (b & 1) * 4 + sp;
  const int cluster = b * 8 + kvh, j32 = kvh * 4 + sp;
  const int q16 = lane & 15, g4 = lane >> 4;

  const __amdgpu_buffer_rsrc_t rws = __builtin_amdgcn_make_buffer_rsrc(p.ws, 0, W_END, 0x00020000);
  const size_t kvbase = ((size_t)cluster * SK + (SK - WIN)) * 512;

  unsigned wof[16][4];

  auto read_w = [&](int seq) {
    const unsigned qq = (lane & 15) >> 2, pq = lane & 3, r = 4 * g4 + qq;
    const unsigned base = L_RING + slot_of(seq) * TILE + r * 128 + (pq & 1) * 8;
#pragma unroll
    for (int u = 0; u < 4; ++u) {
      LAS char* a = smem + (base + (((2 * u + (pq >> 1)) ^ (((r >> 1) & 3) << 1)) << 4));
#pragma unroll
      for (int ht = 0; ht < 4; ++ht) {
        const u32x4 f = __builtin_bit_cast(u32x4, tr_pair(a, ht * 4096, ht * 4096 + 2048));
        wof[ht * 4 + u][0] = f.x; wof[ht * 4 + u][1] = f.y; wof[ht * 4 + u][2] = f.z; wof[ht * 4 + u][3] = f.w;
      }
    }
  };

  if (wave == 4) {
    fl_set(L_FL + lane * 4, 0u);
    fl_set(L_FL + 256 + lane * 4, 0u);
  }

  if (wave >= 6) {
    const __amdgpu_buffer_rsrc_t rk = __builtin_amdgcn_make_buffer_rsrc((void*)(p.kc + kvbase), 0, WIN * 512, 0x00020000);
    const __amdgpu_buffer_rsrc_t rv = __builtin_amdgcn_make_buffer_rsrc((void*)(p.vc + kvbase), 0, WIN * 512, 0x00020000);
    const __amdgpu_buffer_rsrc_t rwo = __builtin_amdgcn_make_buffer_rsrc((void*)p.wo, 0, 4096 * 896, 0x00020000);
    const unsigned worow = (unsigned)((2 * kvh) * 256 + 64 * sp);
    const unsigned hh = lane >> 5, pp = lane & 31;
    const int hf = wave - 6;
    const u64 t0 = __builtin_amdgcn_s_memrealtime();
    auto issue = [&](int s) {
      LAS char* dst = smem + L_RING + slot_of(s) * TILE + hf * 8192;
      const int k = s - 16;
      if (s < 16) {
        const unsigned so = (unsigned)(s * 4 + sp) * TILE + hf * 8192;
#pragma unroll
        for (int i = 0; i < 8; ++i) dma16(rk, dst + i * 1024, hh * 512 + ((pp ^ (2 * i + hh)) << 4), so + i * 1024);
      } else if (is_w(k)) {
        const unsigned r8 = lane >> 3, p8 = lane & 7;
        const unsigned vo = r8 * 896 + (k / 3) * 128 + ((p8 ^ (((r8 >> 1) & 3) << 1)) << 4);
#pragma unroll
        for (int n = 0; n < 8; ++n) dma16(rwo, dst + n * 1024, vo, (worow + hf * 256 + n * 8) * 896);
      } else {
        const unsigned so = (unsigned)(v_of(k) * 4 + sp) * TILE + hf * 8192;
#pragma unroll
        for (int i = 0; i < 8; ++i) dma16(rv, dst + i * 1024, hh * 512 + ((pp ^ (((2 * i + hh) & 7) << 1)) << 4), so + i * 1024);
      }
    };
#pragma unroll
    for (int s = 0; s < LWIN - 1; ++s) issue(s);
    BARRIER_LDS();
    for (int s = LWIN - 1; s < NENT; ++s) {
      const int prev = s - 8;
      if (s >= 8) fl_wait_sleep(F_REL + prev * 4, (prev >= 16 && !is_w(prev - 16)) ? 4u : 1u);
      if (s < 16) {
        const u64 tgt = t0 + (u64)(PACE_OFF + s * PACE);
        for (int n = 0; n < 4096 && __builtin_amdgcn_s_memrealtime() < tgt; ++n) __builtin_amdgcn_s_sleep(1);
      }
      issue(s);
      wait_vm<8 * (LWIN - 1)>();
      fl_inc(F_LAND + (s - (LWIN - 1)) * 4, lane);
    }
    static_assert(LWIN == 5, "the drain below announces the last LWIN - 1 entries");
    wait_vm<24>(); fl_inc(F_LAND + (NENT - 4) * 4, lane);
    wait_vm<16>(); fl_inc(F_LAND + (NENT - 3) * 4, lane);
    wait_vm<8>(); fl_inc(F_LAND + (NENT - 2) * 4, lane);
    wait_vm<0>(); fl_inc(F_LAND + (NENT - 1) * 4, lane);
    if (wave == 6) { fl_wait(F_LAND + seq_w(6) * 4, 2u); read_w(seq_w(6)); }
    else {
#pragma unroll
      for (int i = 0; i < 16; ++i) { wof[i][0] = 0u; wof[i][1] = 0u; wof[i][2] = 0u; wof[i][3] = 0u; }
    }
  } else {
    u32x4 wraw[2][8], xf[2][2];
    u32x2 ep = {0u, 0u};
    if (wave < 4) {
      const __amdgpu_buffer_rsrc_t rx = __builtin_amdgcn_make_buffer_rsrc((void*)(p.x + rg * 16 * 896), 0, 16 * 896, 0x00020000);
      const __amdgpu_buffer_rsrc_t rq = __builtin_amdgcn_make_buffer_rsrc((void*)(p.wq + kvh * 1024 + e8 * 128), 0, 448 * 8192 - (kvh * 1024 + e8 * 128), 0x00020000);
      const unsigned vx = q16 * 896 + g4 * 16, vq = (32 * ((lane >> 3) & 1) + 8 * g4) * 8192 + (lane & 7) * 16;
#pragma unroll
      for (int mm = 0; mm < 2; ++mm) {
        const unsigned m = 2 * wave + mm;
        xf[mm][0] = __builtin_amdgcn_raw_buffer_load_b128(rx, m < 7 ? vx : OOB, m < 7 ? m * 128 : 0, 0);
        xf[mm][1] = __builtin_amdgcn_raw_buffer_load_b128(rx, m < 7 ? vx : OOB, m < 7 ? m * 128 + 64 : 0, 0);
      }
      ep = __builtin_amdgcn_raw_buffer_load_b64(rws, tid == 0 ? W_E + rg * 256 : OOB, 0, AUX_SC1);
#pragma unroll
      for (int mm = 0; mm < 2; ++mm) {
        const unsigned m = 2 * wave + mm;
#pragma unroll
        for (int j = 0; j < 8; ++j) wraw[mm][j] = __builtin_amdgcn_raw_buffer_load_b128(rq, m < 7 ? vq + j * 8192 : OOB, m < 7 ? m * 64 * 8192 : 0, AUX_NT);
      }
    }
    BARRIER_LDS();
    f32x4 sc[3][2];
    bf16x8 qf[8];
    auto load_q = [&]() {
#pragma unroll
      for (int ts = 0; ts < 8; ++ts) qf[ts] = as_frag(lds_ld16(smem, L_QIMG + q16 * 512 + (((4 * ts + g4) ^ q16) << 4)));
    };
    auto park_k = [&](int t, bf16x8 (&kf)[16]) {
      fl_wait(F_LAND + t * 4, 2u);
      const unsigned so = L_RING + slot_of(t) * TILE + q16 * 512;
#pragma unroll
      for (int i = 0; i < 4; ++i) {
        LAS char* kp = smem + (so + (((4 * i + g4) ^ q16) << 4));
        kf[i] = as_frag(lds_ld16(kp, 0)); kf[8 + i] = as_frag(lds_ld16(kp, 8192));
        kf[4 + i] = as_frag(lds_ld16(kp, 256)); kf[12 + i] = as_frag(lds_ld16(kp, 8192 + 256));
      }
      fl_add(F_REL + t * 4, lane);
    };
    auto qk = [&](const bf16x8 (&kf)[16], f32x4 (&s)[2]) {
      f32x4 s0 = {0.f, 0.f, 0.f, 0.f}, s1 = {0.f, 0.f, 0.f, 0.f};
#pragma unroll
      for (int i = 0; i < 8; ++i) { s0 = mfma(kf[i], qf[i], s0); s1 = mfma(kf[8 + i], qf[i], s1); }
      s[0] = s0; s[1] = s1;
    };
    if (wave < 4) {
      {
        f32x4 a0[8], a1[8];
#pragma unroll
        for (int u = 0; u < 8; ++u) { a0[u] = f32x4{0.f, 0.f, 0.f, 0.f}; a1[u] = f32x4{0.f, 0.f, 0.f, 0.f}; }
#pragma unroll
        for (int mm = 0; mm < 2; ++mm)
#pragma unroll
          for (int u = 0; u < 8; ++u) {
            u32x4 bq;
#pragma unroll
            for (int pp = 0; pp < 4; ++pp)
              bq[pp] = __builtin_amdgcn_perm(wraw[mm][2 * pp + 1][u >> 1], wraw[mm][2 * pp][u >> 1], (u & 1) ? 0x07060302u : 0x05040100u);
            a0[u] = mfma(as_frag(xf[mm][0]), as_frag(bq), a0[u]);
            a1[u] = mfma(as_frag(xf[mm][1]), as_frag(bq), a1[u]);
          }
        const bool hx = (lane & 8) != 0;
        const unsigned mx8 = hx ? 0xffffffffu : 0u;
        float r[8][4];
#pragma unroll
        for (int u = 0; u < 8; ++u)
#pragma unroll
          for (int i = 0; i < 4; ++i) { const float own = fsel(mx8, a1[u][i], a0[u][i]); r[u][i] = own + __uint_as_float(dpp<0x128>(__float_as_uint(own))); }
#pragma unroll
        for (int k = 0; k < 2; ++k) {
          LAS char* d = smem + (L_QP + wave * 4096 + (4 * g4 + (hx ? 2 : 0) + k) * 256 + (lane & 7) * 32);
          f32x4 lo, hi;
#pragma unroll
          for (int u = 0; u < 4; ++u) { lo[u] = fsel(mx8, r[u][2 + k], r[u][k]); hi[u] = fsel(mx8, r[4 + u][2 + k], r[4 + u][k]); }
          *(LAS f32x4*)d = lo; *(LAS f32x4*)(d + 16) = hi;
        }
        if (tid == 0) {
          const unsigned tlo = ep.x + 1u, thi = ep.y + ((tlo == 0u) ? 1u : 0u);
          *(LAS u32x4*)(smem + L_T) = u32x4{tlo, thi, tlo, (tlo * 0x9E3779B1u) ^ thi};
        }
      }
      fl_add(C_QSTG, lane);
      if (wave < 2) {
        fl_wait(C_QSTG, 4u);
        const u32x4 tw = *(LAS u32x4*)(smem + L_T);
        const unsigned seedA = __builtin_amdgcn_readfirstlane(tw.z), seedB = __builtin_amdgcn_readfirstlane(tw.w);
        LAS char* d = smem + (L_QP + (8 * wave + (lane >> 3)) * 256 + (lane & 7) * 32);
        f32x4 lo = *(LAS f32x4*)d, hi = *(LAS f32x4*)(d + 16);
#pragma unroll
        for (int w = 1; w < 4; ++w) { lo += *(LAS f32x4*)(d + w * 4096); hi += *(LAS f32x4*)(d + w * 4096 + 16); }
        const u32x4 v = {pack2(lo[0], lo[1]), pack2(lo[2], lo[3]), pack2(hi[0], hi[1]), pack2(hi[2], hi[3])};
        const unsigned piece = ((2 * rg + wave) * 8 + kvh) * 8 + e8;
        __builtin_amdgcn_raw_buffer_store_b128(v, rws, W_Q + piece * 1024 + lane * 16, 0, AUX_SC1);
        const unsigned a = wave_sum(add4(v)) + seedA;
        const unsigned x = wave_xor(xor4(v)) ^ (seedB + e8 * PHI);
        __builtin_amdgcn_raw_buffer_store_b128(u32x4{a, x, seedA, 0u}, rws, lane == 0 ? W_QC + piece * 16 : OOB, 0, AUX_SC1);
      }
      bf16x8 k0[16], k1[16];
      park_k(4 + wave, k0);
      park_k(8 + wave, k1);
      fl_wait(C_QIMG, 2u);
      load_q();
      qk(k0, sc[0]);
      qk(k1, sc[1]);
      park_k(12 + wave, k0);
      qk(k0, sc[2]);
    } else {
      bf16x8 ka[16], kb[16];
      park_k(wave - 4, ka);
      park_k(wave - 2, kb);
      fl_wait_sleep(C_QSTG, 4u);
      const u32x4 tw = *(LAS u32x4*)(smem + L_T);
      const unsigned seedA = __builtin_amdgcn_readfirstlane(tw.z), seedB = __builtin_amdgcn_readfirstlane(tw.w);
      const unsigned p0 = 4 * (wave - 4), pbase = (b * 8 + kvh) * 8 + p0, sxq = seed4(seedB + p0 * PHI);
      u32x4 v[4];
      for (unsigned spins = 0;; ++spins) {
        FENCE();
#pragma unroll
        for (int k = 0; k < 4; ++k) v[k] = __builtin_amdgcn_raw_buffer_load_b128(rws, W_Q + (pbase + k) * 1024 + lane * 16, 0, AUX_SC1);
        const u32x2 c = __builtin_amdgcn_raw_buffer_load_b64(rws, lane < 4 ? W_QC + (pbase + lane) * 16 : OOB, 0, AUX_SC1);
        const unsigned a = wave_sum((add4(v[0]) + add4(v[1])) + (add4(v[2]) + add4(v[3])) - c.x);
        const unsigned x = wave_xor((xor4(v[0]) ^ xor4(v[1])) ^ (xor4(v[2]) ^ xor4(v[3])) ^ c.y);
        const bool ok = (a == 0u - 4u * seedA) && (x == sxq);
        if (__builtin_amdgcn_readfirstlane(ok) || spins > SPIN_CAP) break;
        __builtin_amdgcn_s_sleep(1);
      }
#pragma unroll
      for (int k = 0; k < 4; ++k) {
        const unsigned row = (wave - 4) * 8 + (lane >> 3), c16 = k * 8 + (lane & 7);
        *(LAS u32x4*)(smem + L_QIMG + row * 512 + ((c16 ^ row) << 4)) = v[k];
      }
      fl_add(C_QIMG, lane);
      fl_wait(C_QIMG, 2u);
      load_q();
      qk(ka, sc[0]);
      qk(kb, sc[1]);
      sc[2][0] = f32x4{-INFINITY, -INFINITY, -INFINITY, -INFINITY}; sc[2][1] = sc[2][0];
    }
    float mx = -INFINITY;
#pragma unroll
    for (int i = 0; i < 3; ++i)
#pragma unroll
      for (int k = 0; k < 2; ++k) mx = fmaxf(mx, fmaxf(fmaxf(sc[i][k][0], sc[i][k][1]), fmaxf(sc[i][k][2], sc[i][k][3])));
    mx = fmax32(fmax16(mx));
    if (lane < 16) *(LAS float*)(smem + L_MX + wave * 64 + lane * 4) = mx;
    fl_add(C_MX, lane);
    fl_wait(C_MX, 6u);
    {
      LAS float* pm = (LAS float*)(smem + L_MX + q16 * 4);
      mx = fmaxf(fmaxf(fmaxf(pm[0], pm[16]), fmaxf(pm[32], pm[48])), fmaxf(pm[64], pm[80]));
    }
    const float mc = mx * CSC;
    float ls = 0.f;
#pragma unroll
    for (int i = 0; i < 3; ++i)
#pragma unroll
      for (int k = 0; k < 2; ++k)
#pragma unroll
        for (int e = 0; e < 4; ++e) { const float pe = __builtin_amdgcn_exp2f(sc[i][k][e] * CSC - mc); sc[i][k][e] = pe; ls += pe; }
    ls = fadd32(fadd16(ls));
    if (lane < 16) *(LAS float*)(smem + L_LS + wave * 64 + lane * 4) = ls;
    fl_add(C_LS, lane);
    fl_wait(C_LS, 6u);
    {
      LAS float* pl = (LAS float*)(smem + L_LS + q16 * 4);
      ls = ((pl[0] + pl[16]) + (pl[32] + pl[48])) + (pl[64] + pl[80]);
    }
    const float inv = 1.0f / ls;
#pragma unroll
    for (int i = 0; i < 3; ++i) {
      const int t = wave < 4 ? 4 + 4 * i + wave : (wave - 4) + 2 * i;
      if (wave < 4 || i < 2)
        *(LAS u32x4*)(smem + L_P + t * 1024 + lane * 16) =
            u32x4{pack2(sc[i][0][0] * inv, sc[i][0][1] * inv), pack2(sc[i][0][2] * inv, sc[i][0][3] * inv),
                  pack2(sc[i][1][0] * inv, sc[i][1][1] * inv), pack2(sc[i][1][2] * inv, sc[i][1][3] * inv)};
    }
    fl_add(C_P, lane);
    const u32x4 tw = *(LAS u32x4*)(smem + L_T);
    const unsigned seedA = __builtin_amdgcn_readfirstlane(tw.z), seedB = __builtin_amdgcn_readfirstlane(tw.w);
    if (wave == 4) {
      const unsigned lamb = __float_as_uint(mc + __builtin_amdgcn_logf(ls));
      const unsigned a = row_sum(lamb) + seedA, x = row_xor(lamb) ^ (seedB + sp * PHI);
      const unsigned hb = W_LAM + (cluster * 4 + sp) * 128;
      __builtin_amdgcn_raw_buffer_store_b32(lamb, rws, lane < 16 ? hb + lane * 4 : OOB, 0, AUX_SC1);
      __builtin_amdgcn_raw_buffer_store_b64(u32x2{a, x}, rws, lane == 0 ? hb + 64 : OOB, 0, AUX_SC1);
    }
    if (wave < 4) {
      unsigned vad[4];
#pragma unroll
      for (int i = 0; i < 4; ++i) {
        const unsigned qq = (lane & 15) >> 2, pq = lane & 3, r = 4 * g4 + qq;
        vad[i] = L_RING + r * 512 + (((8 * wave + 2 * i + (pq >> 1)) ^ ((r & 7) << 1)) << 4) + (pq & 1) * 8;
      }
      f32x4 o[4];
#pragma unroll
      for (int j = 0; j < 4; ++j) o[j] = f32x4{0.f, 0.f, 0.f, 0.f};
      fl_wait(C_P, 6u);
      for (int t = 0; t < 16; ++t) {
        const int sq = seq_v(t);
        const u32x4 pf = lds_ld16(smem, L_P + t * 1024 + lane * 16);
        fl_wait(F_LAND + sq * 4, 2u);
        const unsigned so = slot_of(sq) * TILE;
        bf16x8 vf[4];
#pragma unroll
        for (int j = 0; j < 4; ++j) vf[j] = tr_pair(smem + (vad[j] + so), 0, 8192);
        fl_add(F_REL + sq * 4, lane);
#pragma unroll
        for (int j = 0; j < 4; ++j) o[j] = mfma(vf[j], as_frag(pf), o[j]);
        if (t == 2 * wave + 2) { fl_wait(F_LAND + seq_w(wave) * 4, 2u); read_w(seq_w(wave)); fl_add(F_REL + seq_w(wave) * 4, lane); }
      }
      const unsigned ub = W_H1 + ((cluster * 4 + wave) * 4 + sp) * H1_UNIT;
      const u32x4 v0 = {pack2(o[0][0], o[0][1]), pack2(o[0][2], o[0][3]), pack2(o[1][0], o[1][1]), pack2(o[1][2], o[1][3])};
      const u32x4 v1 = {pack2(o[2][0], o[2][1]), pack2(o[2][2], o[2][3]), pack2(o[3][0], o[3][1]), pack2(o[3][2], o[3][3])};
      __builtin_amdgcn_raw_buffer_store_b128(v0, rws, ub + lane * 16, 0, AUX_SC1);
      __builtin_amdgcn_raw_buffer_store_b128(v1, rws, ub + 1024 + lane * 16, 0, AUX_SC1);
      const unsigned a = oct_sum(add4(v0) + add4(v1)) + seedA;
      const unsigned x = oct_xor(xor4(v0) ^ xor4(v1)) ^ (seedB + sp * PHI);
      __builtin_amdgcn_raw_buffer_store_b128(u32x4{a, x, seedA, 0u}, rws, (lane & 7) == 0 ? ub + 2048 + (lane >> 3) * 16 : OOB, 0, AUX_SC1);
    } else {
      fl_wait_sleep(F_LAND + seq_w(wave) * 4, 2u);
      read_w(seq_w(wave));
      fl_add(F_REL + seq_w(wave) * 4, lane);
      if (wave == 5) {
        const unsigned hb = W_LAM + cluster * 4 * 128, sx4 = seed4(seedB);
        unsigned lam;
        for (unsigned spins = 0;; ++spins) {
          FENCE();
          lam = __builtin_amdgcn_raw_buffer_load_b32(rws, hb + g4 * 128 + q16 * 4, 0, AUX_SC1);
          const u32x2 c = __builtin_amdgcn_raw_buffer_load_b64(rws, lane < 4 ? hb + lane * 128 + 64 : OOB, 0, AUX_SC1);
          const bool ok = (wave_sum(lam - c.x) == 0u - 4u * seedA) && (wave_xor(lam ^ c.y) == sx4);
          if (__builtin_amdgcn_readfirstlane(ok) || spins > SPIN_CAP) break;
          __builtin_amdgcn_s_sleep(8);
        }
        const float l = __uint_as_float(lam);
        const float w = __builtin_amdgcn_exp2f(l - fmax32(fmax16(l)));
        *(LAS float*)(smem + L_WGT + q16 * 16 + g4 * 4) = w / fadd32(fadd16(w));
        fl_add(C_WGT, lane);
      }
    }
  }

  const u32x4 tw = *(LAS u32x4*)(smem + L_T);
  const unsigned seedA = __builtin_amdgcn_readfirstlane(tw.z), seedB = __builtin_amdgcn_readfirstlane(tw.w);
  {
    const int gg = wave >> 1, hf = wave & 1;
    const int tt = lane >> 5, s8 = (lane >> 2) & 7, c4 = lane & 3;
    fl_wait(C_WGT, 1u);
    const f32x4 wv = *(LAS f32x4*)(smem + L_WGT + (hf * 8 + s8) * 16);
    const unsigned ub = W_H1 + (cluster * 4 + sp) * 4 * H1_UNIT, sx4 = seed4(seedB);
    const unsigned po = tt * 1024 + (gg * 16 + hf * 8 + s8) * 16 + c4 * 4;
    const unsigned ca = lane < 4 ? ub + lane * H1_UNIT + 2048 + (gg * 2 + hf) * 16 : OOB;
    unsigned pay[4] = {0u, 0u, 0u, 0u};
    unsigned need = 0xFu, seen = 0u;
    for (unsigned spins = 0;; ++spins) {
      FENCE();
      const u32x4 c = __builtin_amdgcn_raw_buffer_load_b128(rws, ca, 0, AUX_SC1);
      unsigned np[4];
#pragma unroll
      for (int s = 0; s < 4; ++s) np[s] = __builtin_amdgcn_raw_buffer_load_b32(rws, (need >> s) & 1u ? ub + s * H1_UNIT + po : OOB, 0, AUX_SC1);
#pragma unroll
      for (int s = 0; s < 4; ++s) pay[s] = (need >> s) & 1u ? np[s] : pay[s];
      const unsigned now = (unsigned)__ballot(c.z == seedA) & 0xFu;
      if (now == 0xFu) {
        const unsigned a = wave_sum((pay[0] + pay[1]) + (pay[2] + pay[3]) - c.x);
        const unsigned x = wave_xor((pay[0] ^ pay[1]) ^ (pay[2] ^ pay[3]) ^ c.y);
        const bool ok = (a == 0u - 4u * seedA) && (x == sx4);
        if (__builtin_amdgcn_readfirstlane(ok) || spins > SPIN_CAP) break;
        need = 0xFu;
      } else {
        if (spins > SPIN_CAP) break;
        need = ~(now & seen) & 0xFu;
        __builtin_amdgcn_s_sleep(2);
      }
      seen = now;
    }
    const float olo = (wv[0] * bflo(pay[0]) + wv[1] * bflo(pay[1])) + (wv[2] * bflo(pay[2]) + wv[3] * bflo(pay[3]));
    const float ohi = (wv[0] * bfhi(pay[0]) + wv[1] * bfhi(pay[1])) + (wv[2] * bfhi(pay[2]) + wv[3] * bfhi(pay[3]));
    const unsigned ov = pack2(olo, ohi);
    LAS unsigned* dst = (LAS unsigned*)(smem + L_OFR + hf * 2048 + tt * 1024 + (gg * 16 + s8) * 16 + c4 * 4);
    dst[0] = ov; dst[32] = ov;
  }
  BARRIER_LDS();
  const unsigned sb = W_H2 + (b * 32 + j32) * H2_UNIT;
  if (wave < 7) {
    bf16x8 of[4];
#pragma unroll
    for (int ht = 0; ht < 4; ++ht) of[ht] = as_frag(lds_ld16(smem + (L_OFR + lane * 16), ht * 1024));
    __builtin_amdgcn_sched_barrier(0);
    f32x4 c[4];
#pragma unroll
    for (int u = 0; u < 4; ++u) c[u] = f32x4{0.f, 0.f, 0.f, 0.f};
#pragma unroll
    for (int ht = 0; ht < 4; ++ht)
#pragma unroll
      for (int u = 0; u < 4; ++u) c[u] = mfma(as_frag(u32x4{wof[ht * 4 + u][0], wof[ht * 4 + u][1], wof[ht * 4 + u][2], wof[ht * 4 + u][3]}), of[ht], c[u]);
    const unsigned msk = q16 >= 8 ? 0xffffffffu : 0u;
    const u32x4 lo4 = {pack2(c[0][0], c[0][1]), pack2(c[0][2], c[0][3]), pack2(c[1][0], c[1][1]), pack2(c[1][2], c[1][3])};
    const u32x4 hi4 = {pack2(c[2][0], c[2][1]), pack2(c[2][2], c[2][3]), pack2(c[3][0], c[3][1]), pack2(c[3][2], c[3][3])};
    const u32x4 v = (hi4 & msk) | (lo4 & ~msk);
    __builtin_amdgcn_raw_buffer_store_b128(v, rws, sb + wave * 1024 + lane * 16, 0, AUX_SC1);
    const unsigned a = row_sum(add4(v)) + seedA;
    const unsigned x = row_xor(xor4(v)) ^ (seedB + j32 * PHI);
    __builtin_amdgcn_raw_buffer_store_b128(u32x4{a, x, seedA, 0u}, rws, q16 == 0 ? sb + 7168 + (wave * 4 + g4) * 16 : OOB, 0, AUX_SC1);
  }
  if (j32 < 28) {
    const int G = j32 >> 2, g = j32 & 3;
    const unsigned bb = W_H2 + b * 32 * H2_UNIT;
    const unsigned po = bb + (4 * wave + g4) * H2_UNIT + G * 1024 + (g * 16 + q16) * 16;
    const unsigned sx = seed4(seedB + (4 * wave) * PHI);
    const unsigned ca = lane < 4 ? bb + (4 * wave + lane) * H2_UNIT + 7168 + j32 * 16 : OOB;
    __builtin_amdgcn_s_sleep(24);
    u32x4 v = {0u, 0u, 0u, 0u};
    unsigned need = 0xFu, seen = 0u;
    for (unsigned spins = 0;; ++spins) {
      FENCE();
      const u32x4 c = __builtin_amdgcn_raw_buffer_load_b128(rws, ca, 0, AUX_SC1);
      const bool mine = (need >> g4) & 1u;
      const u32x4 n = __builtin_amdgcn_raw_buffer_load_b128(rws, mine ? po : OOB, 0, AUX_SC1);
      const unsigned m0 = mine ? 0xffffffffu : 0u;
      v = (n & m0) | (v & ~m0);
      const unsigned now = (unsigned)__ballot(c.z == seedA) & 0xFu;
      if (now == 0xFu) {
        const unsigned a = wave_sum(add4(v) - c.x);
        const unsigned x = wave_xor(xor4(v) ^ c.y);
        const bool ok = (a == 0u - 4u * seedA) && (x == sx);
        if (__builtin_amdgcn_readfirstlane(ok) || spins > SPIN_CAP) break;
        need = 0xFu;
      } else {
        if (spins > SPIN_CAP) break;
        need = ~(now & seen) & 0xFu;
        __builtin_amdgcn_s_sleep(1);
      }
      seen = now;
    }
    f32x4 s0 = {bflo(v.x), bfhi(v.x), bflo(v.y), bfhi(v.y)}, s1 = {bflo(v.z), bfhi(v.z), bflo(v.w), bfhi(v.w)};
#pragma unroll
    for (int u = 0; u < 4; ++u) { s0[u] = fadd32(fadd16(s0[u])); s1[u] = fadd32(fadd16(s1[u])); }
    if (lane < 16) { *(LAS f32x4*)(smem + L_RED + wave * 512 + lane * 32) = s0; *(LAS f32x4*)(smem + L_RED + wave * 512 + lane * 32 + 16) = s1; }
    BARRIER_LDS();
    if (wave == 0 && lane < 32) {
      LAS char* rp = smem + (L_RED + q16 * 32 + (lane >> 4) * 16);
      f32x4 t = *(LAS f32x4*)rp;
#pragma unroll
      for (int w = 1; w < 8; ++w) t += *(LAS f32x4*)(rp + w * 512);
      const int col = 64 * G + 16 * (2 * (q16 >> 3) + (lane >> 4)) + 4 * g;
      *(GAS u32x2*)(p.out + ((size_t)(b * 8 + (q16 & 7)) * DM + col) * 2) = u32x2{pack2(t[0], t[1]), pack2(t[2], t[3])};
      if (j32 == 0 && (b & 1) == 0 && lane == 0)
        __hip_atomic_store((GAS u64*)(p.ws + W_E + rg * 256), ((u64)tw.y << 32) | tw.x, __ATOMIC_RELAXED, __HIP_MEMORY_SCOPE_AGENT);
    }
  }
}

}

extern "C" void kernel_launch(void* const* d_in, const int* in_sizes, int n_in, void* d_out, int out_size, void* d_ws,
                              size_t ws_size, hipStream_t stream) {
  Params p;
  p.x = (const char*)d_in[0];
  p.kc = (const char*)d_in[1];
  p.vc = (const char*)d_in[2];
  p.wq = (const char*)d_in[3];
  p.wo = (const char*)d_in[4];
  p.out = (char*)d_out;
  p.ws = (char*)d_ws;
  (void)hipFuncSetAttribute((const void*)attn_block_kernel, hipFuncAttributeMaxDynamicSharedMemorySize, L_TOTAL);
  hipLaunchKernelGGL(attn_block_kernel, dim3(256), dim3(512), L_TOTAL, stream, p);
}
```

```cpp
#include <hip/hip_runtime.h>
#include <stdint.h>

typedef unsigned long long u64;
typedef unsigned u32x4 __attribute__((ext_vector_type(4)));
typedef unsigned u32x2 __attribute__((ext_vector_type(2)));
typedef float f32x4 __attribute__((ext_vector_type(4)));
typedef __bf16 bf16x8 __attribute__((ext_vector_type(8)));
typedef __bf16 bf16x2 __attribute__((ext_vector_type(2)));
typedef short s16x4 __attribute__((ext_vector_type(4)));
#define LAS __attribute__((address_space(3)))
#define GAS __attribute__((address_space(1)))

namespace {

constexpr int SK = 8192, WIN = 2048, DM = 448;
constexpr int TILE = 16384;
constexpr int NENT = 39;
constexpr int LWIN = 5;
constexpr int PACE = 55, PACE_OFF = 80;
constexpr int L_RING = 0;
constexpr int L_P = 131072;
constexpr int L_QP = L_P;
constexpr int L_QIMG = 147456;
constexpr int L_OFR = L_QIMG;
constexpr int L_RED = L_QIMG + 4096;
constexpr int L_WGT = 155648;
constexpr int L_MX = 157696;
constexpr int L_LS = 158208;
constexpr int L_FL = 158720;
constexpr int F_LAND = L_FL, F_REL = L_FL + 160, C_QSTG = L_FL + 324, C_QIMG = L_FL + 328, C_MX = L_FL + 332,
              C_LS = L_FL + 336, C_P = L_FL + 340, C_WGT = L_FL + 344, C_PUB = L_FL + 348;
constexpr int L_T = L_FL + 512;
constexpr int L_TOTAL = L_T + 16;
constexpr unsigned W_E = 0;
constexpr unsigned W_Q = 4096;
constexpr unsigned W_QC = W_Q + 512 * 1024;
constexpr unsigned W_LAM = W_QC + 8192;
constexpr unsigned W_H1 = W_LAM + 256 * 128;
constexpr unsigned H1_UNIT = 2176;
constexpr unsigned W_H2 = W_H1 + 64 * 16 * H1_UNIT;
constexpr unsigned H2_UNIT = 7680;
constexpr unsigned W_END = W_H2 + 256 * H2_UNIT;
constexpr unsigned OOB = 0x7fffffe0u;
constexpr unsigned SPIN_CAP = 1u << 16, LSPIN_CAP = 1u << 22;
constexpr unsigned PHI = 0x9E3779B9u;
constexpr int AUX_NT = 2, AUX_SC1 = 16;
constexpr float CSC = 0.0625f * 1.4426950408889634f;

struct Params {
  const char* x; const char* kc; const char* vc; const char* wq; const char* wo; char* out; char* ws;
};

template <int CTRL> __device__ __forceinline__ unsigned dpp(unsigned v) { return __builtin_amdgcn_update_dpp(0u, v, CTRL, 0xf, 0xf, true); }
__device__ __forceinline__ unsigned row_sum(unsigned v) { v += dpp<0x128>(v); v += dpp<0x124>(v); v += dpp<0x122>(v); v += dpp<0x121>(v); return v; }
__device__ __forceinline__ unsigned row_xor(unsigned v) { v ^= dpp<0x128>(v); v ^= dpp<0x124>(v); v ^= dpp<0x122>(v); v ^= dpp<0x121>(v); return v; }
__device__ __forceinline__ unsigned oct_sum(unsigned v) { v += dpp<0xB1>(v); v += dpp<0x4E>(v); v += dpp<0x141>(v); return v; }
__device__ __forceinline__ unsigned oct_xor(unsigned v) { v ^= dpp<0xB1>(v); v ^= dpp<0x4E>(v); v ^= dpp<0x141>(v); return v; }
__device__ __forceinline__ unsigned sum16(unsigned v) { auto s = __builtin_amdgcn_permlane16_swap(v, v, false, false); return s[0] + s[1]; }
__device__ __forceinline__ unsigned sum32(unsigned v) { auto s = __builtin_amdgcn_permlane32_swap(v, v, false, false); return s[0] + s[1]; }
__device__ __forceinline__ unsigned xor16(unsigned v) { auto s = __builtin_amdgcn_permlane16_swap(v, v, false, false); return s[0] ^ s[1]; }
__device__ __forceinline__ unsigned xor32(unsigned v) { auto s = __builtin_amdgcn_permlane32_swap(v, v, false, false); return s[0] ^ s[1]; }
__device__ __forceinline__ unsigned wave_sum(unsigned v) { return sum32(sum16(row_sum(v))); }
__device__ __forceinline__ unsigned wave_xor(unsigned v) { return xor32(xor16(row_xor(v))); }
__device__ __forceinline__ float fmax16(float v) { auto s = __builtin_amdgcn_permlane16_swap(__float_as_uint(v), __float_as_uint(v), false, false); return fmaxf(__uint_as_float(s[0]), __uint_as_float(s[1])); }
__device__ __forceinline__ float fmax32(float v) { auto s = __builtin_amdgcn_permlane32_swap(__float_as_uint(v), __float_as_uint(v), false, false); return fmaxf(__uint_as_float(s[0]), __uint_as_float(s[1])); }
__device__ __forceinline__ float fadd16(float v) { auto s = __builtin_amdgcn_permlane16_swap(__float_as_uint(v), __float_as_uint(v), false, false); return __uint_as_float(s[0]) + __uint_as_float(s[1]); }
__device__ __forceinline__ float fadd32(float v) { auto s = __builtin_amdgcn_permlane32_swap(__float_as_uint(v), __float_as_uint(v), false, false); return __uint_as_float(s[0]) + __uint_as_float(s[1]); }
__device__ __forceinline__ unsigned pack2(float a, float b) { bf16x2 t = {(__bf16)a, (__bf16)b}; return __builtin_bit_cast(unsigned, t); }
__device__ __forceinline__ float bflo(unsigned v) { return __uint_as_float(v << 16); }
__device__ __forceinline__ float bfhi(unsigned v) { return __uint_as_float(v & 0xffff0000u); }
__device__ __forceinline__ unsigned xor4(u32x4 v) { return (v.x ^ v.y) ^ (v.z ^ v.w); }
__device__ __forceinline__ unsigned add4(u32x4 v) { return (v.x + v.y) + (v.z + v.w); }
__device__ __forceinline__ float fsel(unsigned m, float a, float b) { return __uint_as_float((__float_as_uint(a) & m) | (__float_as_uint(b) & ~m)); }
__device__ __forceinline__ unsigned seed4(unsigned k0) { return (k0 ^ (k0 + PHI)) ^ ((k0 + 2u * PHI) ^ (k0 + 3u * PHI)); }
__device__ __forceinline__ f32x4 mfma(bf16x8 a, bf16x8 b, f32x4 c) { return __builtin_amdgcn_mfma_f32_16x16x32_bf16(a, b, c, 0, 0, 0); }
__device__ __forceinline__ bf16x8 as_frag(u32x4 v) { return __builtin_bit_cast(bf16x8, v); }
__device__ __forceinline__ u32x4 lds_ld16(LAS char* base, unsigned off) { return *(LAS u32x4*)(base + off); }
__device__ __forceinline__ bf16x8 tr_pair(LAS char* base, unsigned off0, unsigned off1) {
  s16x4 a = __builtin_amdgcn_ds_read_tr16_b64_v4i16((LAS s16x4*)(base + off0));
  s16x4 b = __builtin_amdgcn_ds_read_tr16_b64_v4i16((LAS s16x4*)(base + off1));
  return __builtin_bit_cast(bf16x8, __builtin_shufflevector(a, b, 0, 1, 2, 3, 4, 5, 6, 7));
}
#define BARRIER_LDS() asm volatile("s_waitcnt lgkmcnt(0)\n\ts_barrier" ::: "memory")
template <int N> __device__ __forceinline__ void wait_vm() { asm volatile("s_waitcnt vmcnt(%0)" ::"n"(N) : "memory"); }
#define FENCE() asm volatile("" ::: "memory")

__device__ __forceinline__ void fl_set(unsigned addr, unsigned v) { asm volatile("ds_write_b32 %0, %1" ::"v"(addr), "v"(v) : "memory"); }
__device__ __forceinline__ void fl_inc(unsigned addr, int lane) {
  if (lane == 0) asm volatile("ds_add_u32 %0, %1" ::"v"(addr), "v"(1u) : "memory");
}
__device__ __forceinline__ void fl_add(unsigned addr, int lane) {
  asm volatile("s_waitcnt lgkmcnt(0)" ::: "memory");
  fl_inc(addr, lane);
}
__device__ __forceinline__ unsigned fl_get(unsigned addr) {
  unsigned r; asm volatile("ds_read_b32 %0, %1\n\ts_waitcnt lgkmcnt(0)" : "=v"(r) : "v"(addr) : "memory");
  return __builtin_amdgcn_readfirstlane(r);
}
__device__ __forceinline__ void fl_wait(unsigned addr, unsigned want) {
  for (unsigned n = 0; fl_get(addr) < want && n < LSPIN_CAP; ++n) { }
}
__device__ __forceinline__ void fl_wait_sleep(unsigned addr, unsigned want) {
  for (unsigned n = 0; fl_get(addr) < want && n < LSPIN_CAP; ++n) __builtin_amdgcn_s_sleep(1);
}

__device__ __forceinline__ int slot_of(int seq) { return seq & 7; }
__device__ __forceinline__ int seq_v(int t) { return 16 + t + ((t >> 1) < 7 ? (t >> 1) : 7); }
__device__ __forceinline__ int seq_w(int g) { return 18 + 3 * g; }
__device__ __forceinline__ bool is_w(int k) { return k % 3 == 2 && k < 21; }
__device__ __forceinline__ int v_of(int k) { return k < 21 ? k - (k + 1) / 3 : k - 7; }
__device__ __forceinline__ void dma16(__amdgpu_buffer_rsrc_t r, LAS char* lds, unsigned voff, unsigned soff) {
  __builtin_amdgcn_raw_ptr_buffer_load_lds(r, (LAS void*)lds, 16, voff, soff, 0, AUX_NT);
}

__global__ void __launch_bounds__(512, 2) attn_block_kernel(Params p) {
  extern __shared__ __attribute__((aligned(16))) char smem_[];
  LAS char* smem = (LAS char*)smem_;
  const int tid = threadIdx.x, lane = tid & 63;
  const int wave = __builtin_amdgcn_readfirstlane(tid >> 6);
  const int wg = blockIdx.x, xc = wg & 7, ii = wg >> 3;
  const int sp = (xc & 1) | ((xc >> 2) << 1);
  const int kvh = ((xc >> 1) & 1) | ((ii & 3) << 1);
  const int b = ii >> 2;
  const int rg = b >> 1, e8 = (b & 1) * 4 + sp;
  const int cluster = b * 8 + kvh, j32 = kvh * 4 + sp;
  const int q16 = lane & 15, g4 = lane >> 4;

  const __amdgpu_buffer_rsrc_t rws = __builtin_amdgcn_make_buffer_rsrc(p.ws, 0, W_END, 0x00020000);
  const size_t kvbase = ((size_t)cluster * SK + (SK - WIN)) * 512;

  unsigned wof[16][4];

  auto read_w = [&](int seq) {
    const unsigned qq = (lane & 15) >> 2, pq = lane & 3, r = 4 * g4 + qq;
    const unsigned base = L_RING + slot_of(seq) * TILE + r * 128 + (pq & 1) * 8;
#pragma unroll
    for (int u = 0; u < 4; ++u) {
      LAS char* a = smem + (base + (((2 * u + (pq >> 1)) ^ (((r >> 1) & 3) << 1)) << 4));
#pragma unroll
      for (int ht = 0; ht < 4; ++ht) {
        const u32x4 f = __builtin_bit_cast(u32x4, tr_pair(a, ht * 4096, ht * 4096 + 2048));
        wof[ht * 4 + u][0] = f.x; wof[ht * 4 + u][1] = f.y; wof[ht * 4 + u][2] = f.z; wof[ht * 4 + u][3] = f.w;
      }
    }
  };

  if (wave == 4) {
    fl_set(L_FL + lane * 4, 0u);
    fl_set(L_FL + 256 + lane * 4, 0u);
  }

  if (wave >= 6) {
    const __amdgpu_buffer_rsrc_t rk = __builtin_amdgcn_make_buffer_rsrc((void*)(p.kc + kvbase), 0, WIN * 512, 0x00020000);
    const __amdgpu_buffer_rsrc_t rv = __builtin_amdgcn_make_buffer_rsrc((void*)(p.vc + kvbase), 0, WIN * 512, 0x00020000);
    const __amdgpu_buffer_rsrc_t rwo = __builtin_amdgcn_make_buffer_rsrc((void*)p.wo, 0, 4096 * 896, 0x00020000);
    const unsigned worow = (unsigned)((2 * kvh) * 256 + 64 * sp);
    const unsigned hh = lane >> 5, pp = lane & 31;
    const int hf = wave - 6;
    const u64 t0 = __builtin_amdgcn_s_memrealtime();
    auto issue = [&](int s) {
      LAS char* dst = smem + L_RING + slot_of(s) * TILE + hf * 8192;
      const int k = s - 16;
      if (s < 16) {
        const unsigned so = (unsigned)(s * 4 + sp) * TILE + hf * 8192;
#pragma unroll
        for (int i = 0; i < 8; ++i) dma16(rk, dst + i * 1024, hh * 512 + ((pp ^ (2 * i + hh)) << 4), so + i * 1024);
      } else if (is_w(k)) {
        const unsigned r8 = lane >> 3, p8 = lane & 7;
        const unsigned vo = r8 * 896 + (k / 3) * 128 + ((p8 ^ (((r8 >> 1) & 3) << 1)) << 4);
#pragma unroll
        for (int n = 0; n < 8; ++n) dma16(rwo, dst + n * 1024, vo, (worow + hf * 256 + n * 8) * 896);
      } else {
        const unsigned so = (unsigned)(v_of(k) * 4 + sp) * TILE + hf * 8192;
#pragma unroll
        for (int i = 0; i < 8; ++i) dma16(rv, dst + i * 1024, hh * 512 + ((pp ^ (((2 * i + hh) & 7) << 1)) << 4), so + i * 1024);
      }
    };
#pragma unroll
    for (int s = 0; s < LWIN - 1; ++s) issue(s);
    BARRIER_LDS();
    for (int s = LWIN - 1; s < NENT; ++s) {
      const int prev = s - 8;
      if (s >= 8) fl_wait_sleep(F_REL + prev * 4, (prev >= 16 && !is_w(prev - 16)) ? 4u : 1u);
      if (s < 16) {
        const u64 tgt = t0 + (u64)(PACE_OFF + s * PACE);
        for (int n = 0; n < 4096 && __builtin_amdgcn_s_memrealtime() < tgt; ++n) __builtin_amdgcn_s_sleep(1);
      }
      issue(s);
      wait_vm<8 * (LWIN - 1)>();
      fl_inc(F_LAND + (s - (LWIN - 1)) * 4, lane);
    }
    static_assert(LWIN == 5, "the drain below announces the last LWIN - 1 entries");
    wait_vm<24>(); fl_inc(F_LAND + (NENT - 4) * 4, lane);
    wait_vm<16>(); fl_inc(F_LAND + (NENT - 3) * 4, lane);
    wait_vm<8>(); fl_inc(F_LAND + (NENT - 2) * 4, lane);
    wait_vm<0>(); fl_inc(F_LAND + (NENT - 1) * 4, lane);
    if (wave == 6) { fl_wait(F_LAND + seq_w(6) * 4, 2u); read_w(seq_w(6)); }
    else {
#pragma unroll
      for (int i = 0; i < 16; ++i) { wof[i][0] = 0u; wof[i][1] = 0u; wof[i][2] = 0u; wof[i][3] = 0u; }
    }
  } else {
    u32x4 wraw[2][8], xf[2][2];
    u32x2 ep = {0u, 0u};
    if (wave < 4) {
      const __amdgpu_buffer_rsrc_t rx = __builtin_amdgcn_make_buffer_rsrc((void*)(p.x + rg * 16 * 896), 0, 16 * 896, 0x00020000);
      const __amdgpu_buffer_rsrc_t rq = __builtin_amdgcn_make_buffer_rsrc((void*)(p.wq + kvh * 1024 + e8 * 128), 0, 448 * 8192 - (kvh * 1024 + e8 * 128), 0x00020000);
      const unsigned vx = q16 * 896 + g4 * 16, vq = (32 * ((lane >> 3) & 1) + 8 * g4) * 8192 + (lane & 7) * 16;
#pragma unroll
      for (int mm = 0; mm < 2; ++mm) {
        const unsigned m = 2 * wave + mm;
        xf[mm][0] = __builtin_amdgcn_raw_buffer_load_b128(rx, m < 7 ? vx : OOB, m < 7 ? m * 128 : 0, 0);
        xf[mm][1] = __builtin_amdgcn_raw_buffer_load_b128(rx, m < 7 ? vx : OOB, m < 7 ? m * 128 + 64 : 0, 0);
      }
      ep = __builtin_amdgcn_raw_buffer_load_b64(rws, tid == 0 ? W_E + rg * 256 : OOB, 0, AUX_SC1);
#pragma unroll
      for (int mm = 0; mm < 2; ++mm) {
        const unsigned m = 2 * wave + mm;
#pragma unroll
        for (int j = 0; j < 8; ++j) wraw[mm][j] = __builtin_amdgcn_raw_buffer_load_b128(rq, m < 7 ? vq + j * 8192 : OOB, m < 7 ? m * 64 * 8192 : 0, AUX_NT);
      }
    }
    BARRIER_LDS();
    f32x4 sc[3][2];
    bf16x8 qf[8];
    auto load_q = [&]() {
#pragma unroll
      for (int ts = 0; ts < 8; ++ts) qf[ts] = as_frag(lds_ld16(smem, L_QIMG + q16 * 512 + (((4 * ts + g4) ^ q16) << 4)));
    };
    auto park_k = [&](int t, bf16x8 (&kf)[16]) {
      fl_wait(F_LAND + t * 4, 2u);
      const unsigned so = L_RING + slot_of(t) * TILE + q16 * 512;
#pragma unroll
      for (int i = 0; i < 4; ++i) {
        LAS char* kp = smem + (so + (((4 * i + g4) ^ q16) << 4));
        kf[i] = as_frag(lds_ld16(kp, 0)); kf[8 + i] = as_frag(lds_ld16(kp, 8192));
        kf[4 + i] = as_frag(lds_ld16(kp, 256)); kf[12 + i] = as_frag(lds_ld16(kp, 8192 + 256));
      }
      fl_add(F_REL + t * 4, lane);
    };
    auto qk = [&](const bf16x8 (&kf)[16], f32x4 (&s)[2]) {
      f32x4 s0 = {0.f, 0.f, 0.f, 0.f}, s1 = {0.f, 0.f, 0.f, 0.f};
#pragma unroll
      for (int i = 0; i < 8; ++i) { s0 = mfma(kf[i], qf[i], s0); s1 = mfma(kf[8 + i], qf[i], s1); }
      s[0] = s0; s[1] = s1;
    };
    if (wave < 4) {
      {
        f32x4 a0[8], a1[8];
#pragma unroll
        for (int u = 0; u < 8; ++u) { a0[u] = f32x4{0.f, 0.f, 0.f, 0.f}; a1[u] = f32x4{0.f, 0.f, 0.f, 0.f}; }
#pragma unroll
        for (int mm = 0; mm < 2; ++mm)
#pragma unroll
          for (int u = 0; u < 8; ++u) {
            u32x4 bq;
#pragma unroll
            for (int pp = 0; pp < 4; ++pp)
              bq[pp] = __builtin_amdgcn_perm(wraw[mm][2 * pp + 1][u >> 1], wraw[mm][2 * pp][u >> 1], (u & 1) ? 0x07060302u : 0x05040100u);
            a0[u] = mfma(as_frag(xf[mm][0]), as_frag(bq), a0[u]);
            a1[u] = mfma(as_frag(xf[mm][1]), as_frag(bq), a1[u]);
          }
        const bool hx = (lane & 8) != 0;
        const unsigned mx8 = hx ? 0xffffffffu : 0u;
        float r[8][4];
#pragma unroll
        for (int u = 0; u < 8; ++u)
#pragma unroll
          for (int i = 0; i < 4; ++i) { const float own = fsel(mx8, a1[u][i], a0[u][i]); r[u][i] = own + __uint_as_float(dpp<0x128>(__float_as_uint(own))); }
#pragma unroll
        for (int k = 0; k < 2; ++k) {
          LAS char* d = smem + (L_QP + wave * 4096 + (4 * g4 + (hx ? 2 : 0) + k) * 256 + (lane & 7) * 32);
          f32x4 lo, hi;
#pragma unroll
          for (int u = 0; u < 4; ++u) { lo[u] = fsel(mx8, r[u][2 + k], r[u][k]); hi[u] = fsel(mx8, r[4 + u][2 + k], r[4 + u][k]); }
          *(LAS f32x4*)d = lo; *(LAS f32x4*)(d + 16) = hi;
        }
        if (tid == 0) {
          const unsigned tlo = ep.x + 1u, thi = ep.y + ((tlo == 0u) ? 1u : 0u);
          *(LAS u32x4*)(smem + L_T) = u32x4{tlo, thi, tlo, (tlo * 0x9E3779B1u) ^ thi};
        }
      }
      fl_add(C_QSTG, lane);
      if (wave < 2) {
        fl_wait(C_QSTG, 4u);
        const u32x4 tw = *(LAS u32x4*)(smem + L_T);
        const unsigned seedA = __builtin_amdgcn_readfirstlane(tw.z), seedB = __builtin_amdgcn_readfirstlane(tw.w);
        LAS char* d = smem + (L_QP + (8 * wave + (lane >> 3)) * 256 + (lane & 7) * 32);
        f32x4 lo = *(LAS f32x4*)d, hi = *(LAS f32x4*)(d + 16);
#pragma unroll
        for (int w = 1; w < 4; ++w) { lo += *(LAS f32x4*)(d + w * 4096); hi += *(LAS f32x4*)(d + w * 4096 + 16); }
        const u32x4 v = {pack2(lo[0], lo[1]), pack2(lo[2], lo[3]), pack2(hi[0], hi[1]), pack2(hi[2], hi[3])};
        const unsigned piece = ((2 * rg + wave) * 8 + kvh) * 8 + e8;
        __builtin_amdgcn_raw_buffer_store_b128(v, rws, W_Q + piece * 1024 + lane * 16, 0, AUX_SC1);
        const unsigned a = wave_sum(add4(v)) + seedA;
        const unsigned x = wave_xor(xor4(v)) ^ (seedB + e8 * PHI);
        __builtin_amdgcn_raw_buffer_store_b128(u32x4{a, x, seedA, 0u}, rws, lane == 0 ? W_QC + piece * 16 : OOB, 0, AUX_SC1);
      }
      bf16x8 k0[16], k1[16];
      park_k(4 + wave, k0);
      park_k(8 + wave, k1);
      fl_wait(C_QIMG, 2u);
      load_q();
      qk(k0, sc[0]);
      qk(k1, sc[1]);
      park_k(12 + wave, k0);
      qk(k0, sc[2]);
    } else {
      bf16x8 ka[16], kb[16];
      park_k(wave - 4, ka);
      park_k(wave - 2, kb);
      fl_wait_sleep(C_QSTG, 4u);
      const u32x4 tw = *(LAS u32x4*)(smem + L_T);
      const unsigned seedA = __builtin_amdgcn_readfirstlane(tw.z), seedB = __builtin_amdgcn_readfirstlane(tw.w);
      const unsigned p0 = 4 * (wave - 4), pbase = (b * 8 + kvh) * 8 + p0, sxq = seed4(seedB + p0 * PHI);
      u32x4 v[4];
      for (unsigned spins = 0;; ++spins) {
        FENCE();
#pragma unroll
        for (int k = 0; k < 4; ++k) v[k] = __builtin_amdgcn_raw_buffer_load_b128(rws, W_Q + (pbase + k) * 1024 + lane * 16, 0, AUX_SC1);
        const u32x2 c = __builtin_amdgcn_raw_buffer_load_b64(rws, lane < 4 ? W_QC + (pbase + lane) * 16 : OOB, 0, AUX_SC1);
        const unsigned a = wave_sum((add4(v[0]) + add4(v[1])) + (add4(v[2]) + add4(v[3])) - c.x);
        const unsigned x = wave_xor((xor4(v[0]) ^ xor4(v[1])) ^ (xor4(v[2]) ^ xor4(v[3])) ^ c.y);
        const bool ok = (a == 0u - 4u * seedA) && (x == sxq);
        if (__builtin_amdgcn_readfirstlane(ok) || spins > SPIN_CAP) break;
        __builtin_amdgcn_s_sleep(1);
      }
#pragma unroll
      for (int k = 0; k < 4; ++k) {
        const unsigned row = (wave - 4) * 8 + (lane >> 3), c16 = k * 8 + (lane & 7);
        *(LAS u32x4*)(smem + L_QIMG + row * 512 + ((c16 ^ row) << 4)) = v[k];
      }
      fl_add(C_QIMG, lane);
      fl_wait(C_QIMG, 2u);
      load_q();
      qk(ka, sc[0]);
      qk(kb, sc[1]);
      sc[2][0] = f32x4{-INFINITY, -INFINITY, -INFINITY, -INFINITY}; sc[2][1] = sc[2][0];
    }
#pragma unroll
    for (int i = 0; i < 3; ++i)
#pragma unroll
      for (int k = 0; k < 2; ++k) {
        const unsigned lo = pack2(sc[i][k][0], sc[i][k][1]), hi = pack2(sc[i][k][2], sc[i][k][3]);
        sc[i][k] = f32x4{bflo(lo), bfhi(lo), bflo(hi), bfhi(hi)};
      }
    float mx = -INFINITY;
#pragma unroll
    for (int i = 0; i < 3; ++i)
#pragma unroll
      for (int k = 0; k < 2; ++k) mx = fmaxf(mx, fmaxf(fmaxf(sc[i][k][0], sc[i][k][1]), fmaxf(sc[i][k][2], sc[i][k][3])));
    mx = fmax32(fmax16(mx));
    if (lane < 16) *(LAS float*)(smem + L_MX + wave * 64 + lane * 4) = mx;
    fl_add(C_MX, lane);
    fl_wait(C_MX, 6u);
    {
      LAS float* pm = (LAS float*)(smem + L_MX + q16 * 4);
      mx = fmaxf(fmaxf(fmaxf(pm[0], pm[16]), fmaxf(pm[32], pm[48])), fmaxf(pm[64], pm[80]));
    }
    const float mc = mx * CSC;
    float ls = 0.f;
#pragma unroll
    for (int i = 0; i < 3; ++i)
#pragma unroll
      for (int k = 0; k < 2; ++k)
#pragma unroll
        for (int e = 0; e < 4; ++e) { const float pe = __builtin_amdgcn_exp2f(sc[i][k][e] * CSC - mc); sc[i][k][e] = pe; ls += pe; }
    ls = fadd32(fadd16(ls));
    if (lane < 16) *(LAS float*)(smem + L_LS + wave * 64 + lane * 4) = ls;
    fl_add(C_LS, lane);
    fl_wait(C_LS, 6u);
    {
      LAS float* pl = (LAS float*)(smem + L_LS + q16 * 4);
      ls = ((pl[0] + pl[16]) + (pl[32] + pl[48])) + (pl[64] + pl[80]);
    }
    const float inv = 1.0f / ls;
#pragma unroll
    for (int i = 0; i < 3; ++i) {
      const int t = wave < 4 ? 4 + 4 * i + wave : (wave - 4) + 2 * i;
      if (wave < 4 || i < 2)
        *(LAS u32x4*)(smem + L_P + t * 1024 + lane * 16) =
            u32x4{pack2(sc[i][0][0] * inv, sc[i][0][1] * inv), pack2(sc[i][0][2] * inv, sc[i][0][3] * inv),
                  pack2(sc[i][1][0] * inv, sc[i][1][1] * inv), pack2(sc[i][1][2] * inv, sc[i][1][3] * inv)};
    }
    fl_add(C_P, lane);
    const u32x4 tw = *(LAS u32x4*)(smem + L_T);
    const unsigned seedA = __builtin_amdgcn_readfirstlane(tw.z), seedB = __builtin_amdgcn_readfirstlane(tw.w);
    if (wave == 4) {
      const unsigned lamb = __float_as_uint(mc + __builtin_amdgcn_logf(ls));
      const unsigned a = row_sum(lamb) + seedA, x = row_xor(lamb) ^ (seedB + sp * PHI);
      const unsigned hb = W_LAM + (cluster * 4 + sp) * 128;
      __builtin_amdgcn_raw_buffer_store_b32(lamb, rws, lane < 16 ? hb + lane * 4 : OOB, 0, AUX_SC1);
      __builtin_amdgcn_raw_buffer_store_b64(u32x2{a, x}, rws, lane == 0 ? hb + 64 : OOB, 0, AUX_SC1);
    }
    if (wave < 4) {
      unsigned vad[4];
#pragma unroll
      for (int i = 0; i < 4; ++i) {
        const unsigned qq = (lane & 15) >> 2, pq = lane & 3, r = 4 * g4 + qq;
        vad[i] = L_RING + r * 512 + (((8 * wave + 2 * i + (pq >> 1)) ^ ((r & 7) << 1)) << 4) + (pq & 1) * 8;
      }
      f32x4 o[4];
#pragma unroll
      for (int j = 0; j < 4; ++j) o[j] = f32x4{0.f, 0.f, 0.f, 0.f};
      fl_wait(C_P, 6u);
      for (int t = 0; t < 16; ++t) {
        const int sq = seq_v(t);
        const u32x4 pf = lds_ld16(smem, L_P + t * 1024 + lane * 16);
        fl_wait(F_LAND + sq * 4, 2u);
        const unsigned so = slot_of(sq) * TILE;
        bf16x8 vf[4];
#pragma unroll
        for (int j = 0; j < 4; ++j) vf[j] = tr_pair(smem + (vad[j] + so), 0, 8192);
        fl_add(F_REL + sq * 4, lane);
#pragma unroll
        for (int j = 0; j < 4; ++j) o[j] = mfma(vf[j], as_frag(pf), o[j]);
        if (t == 2 * wave + 2) { fl_wait(F_LAND + seq_w(wave) * 4, 2u); read_w(seq_w(wave)); fl_add(F_REL + seq_w(wave) * 4, lane); }
      }
      const unsigned ub = W_H1 + ((cluster * 4 + wave) * 4 + sp) * H1_UNIT;
      const u32x4 v0 = {pack2(o[0][0], o[0][1]), pack2(o[0][2], o[0][3]), pack2(o[1][0], o[1][1]), pack2(o[1][2], o[1][3])};
      const u32x4 v1 = {pack2(o[2][0], o[2][1]), pack2(o[2][2], o[2][3]), pack2(o[3][0], o[3][1]), pack2(o[3][2], o[3][3])};
      __builtin_amdgcn_raw_buffer_store_b128(v0, rws, ub + lane * 16, 0, AUX_SC1);
      __builtin_amdgcn_raw_buffer_store_b128(v1, rws, ub + 1024 + lane * 16, 0, AUX_SC1);
      const unsigned a = oct_sum(add4(v0) + add4(v1)) + seedA;
      const unsigned x = oct_xor(xor4(v0) ^ xor4(v1)) ^ (seedB + sp * PHI);
      __builtin_amdgcn_raw_buffer_store_b128(u32x4{a, x, seedA, 0u}, rws, (lane & 7) == 0 ? ub + 2048 + (lane >> 3) * 16 : OOB, 0, AUX_SC1);
      fl_inc(C_PUB, lane);
    } else {
      fl_wait_sleep(F_LAND + seq_w(wave) * 4, 2u);
      read_w(seq_w(wave));
      fl_add(F_REL + seq_w(wave) * 4, lane);
      if (wave == 5) {
        const unsigned hb = W_LAM + cluster * 4 * 128, sx4 = seed4(seedB);
        unsigned lam;
        for (unsigned spins = 0;; ++spins) {
          FENCE();
          lam = __builtin_amdgcn_raw_buffer_load_b32(rws, hb + g4 * 128 + q16 * 4, 0, AUX_SC1);
          const u32x2 c = __builtin_amdgcn_raw_buffer_load_b64(rws, lane < 4 ? hb + lane * 128 + 64 : OOB, 0, AUX_SC1);
          const bool ok = (wave_sum(lam - c.x) == 0u - 4u * seedA) && (wave_xor(lam ^ c.y) == sx4);
          if (__builtin_amdgcn_readfirstlane(ok) || spins > SPIN_CAP) break;
          __builtin_amdgcn_s_sleep(8);
        }
        const float l = __uint_as_float(lam);
        const float w = __builtin_amdgcn_exp2f(l - fmax32(fmax16(l)));
        *(LAS float*)(smem + L_WGT + q16 * 16 + g4 * 4) = w / fadd32(fadd16(w));
        fl_add(C_WGT, lane);
      }
    }
  }

  const u32x4 tw = *(LAS u32x4*)(smem + L_T);
  const unsigned seedA = __builtin_amdgcn_readfirstlane(tw.z), seedB = __builtin_amdgcn_readfirstlane(tw.w);
  {
    const int gg = wave >> 1, hf = wave & 1;
    const int tt = lane >> 5, s8 = (lane >> 2) & 7, c4 = lane & 3;
    fl_wait(C_WGT, 1u);
    const f32x4 wv = *(LAS f32x4*)(smem + L_WGT + (hf * 8 + s8) * 16);
    if (wave >= 4) fl_wait_sleep(C_PUB, 1u);
    const unsigned ub = W_H1 + (cluster * 4 + sp) * 4 * H1_UNIT, sx4 = seed4(seedB);
    const unsigned po = tt * 1024 + (gg * 16 + hf * 8 + s8) * 16 + c4 * 4;
    const unsigned ca = lane < 4 ? ub + lane * H1_UNIT + 2048 + (gg * 2 + hf) * 16 : OOB;
    unsigned pay[4] = {0u, 0u, 0u, 0u};
    unsigned need = 0xFu, seen = 0u;
    for (unsigned spins = 0;; ++spins) {
      FENCE();
      const u32x4 c = __builtin_amdgcn_raw_buffer_load_b128(rws, ca, 0, AUX_SC1);
      unsigned np[4];
#pragma unroll
      for (int s = 0; s < 4; ++s) np[s] = __builtin_amdgcn_raw_buffer_load_b32(rws, (need >> s) & 1u ? ub + s * H1_UNIT + po : OOB, 0, AUX_SC1);
#pragma unroll
      for (int s = 0; s < 4; ++s) pay[s] = (need >> s) & 1u ? np[s] : pay[s];
      const unsigned now = (unsigned)__ballot(c.z == seedA) & 0xFu;
      if (now == 0xFu) {
        const unsigned a = wave_sum((pay[0] + pay[1]) + (pay[2] + pay[3]) - c.x);
        const unsigned x = wave_xor((pay[0] ^ pay[1]) ^ (pay[2] ^ pay[3]) ^ c.y);
        const bool ok = (a == 0u - 4u * seedA) && (x == sx4);
        if (__builtin_amdgcn_readfirstlane(ok) || spins > SPIN_CAP) break;
        need = 0xFu;
      } else {
        if (spins > SPIN_CAP) break;
        need = ~(now & seen) & 0xFu;
        __builtin_amdgcn_s_sleep(2);
      }
      seen = now;
    }
    const float olo = (wv[0] * bflo(pay[0]) + wv[1] * bflo(pay[1])) + (wv[2] * bflo(pay[2]) + wv[3] * bflo(pay[3]));
    const float ohi = (wv[0] * bfhi(pay[0]) + wv[1] * bfhi(pay[1])) + (wv[2] * bfhi(pay[2]) + wv[3] * bfhi(pay[3]));
    const unsigned ov = pack2(olo, ohi);
    LAS unsigned* dst = (LAS unsigned*)(smem + L_OFR + hf * 2048 + tt * 1024 + (gg * 16 + s8) * 16 + c4 * 4);
    dst[0] = ov; dst[32] = ov;
  }
  BARRIER_LDS();
  const unsigned sb = W_H2 + (b * 32 + j32) * H2_UNIT;
  if (wave < 7) {
    bf16x8 of[4];
#pragma unroll
    for (int ht = 0; ht < 4; ++ht) of[ht] = as_frag(lds_ld16(smem + (L_OFR + lane * 16), ht * 1024));
    __builtin_amdgcn_sched_barrier(0);
    f32x4 c[4];
#pragma unroll
    for (int u = 0; u < 4; ++u) c[u] = f32x4{0.f, 0.f, 0.f, 0.f};
#pragma unroll
    for (int ht = 0; ht < 4; ++ht)
#pragma unroll
      for (int u = 0; u < 4; ++u) c[u] = mfma(as_frag(u32x4{wof[ht * 4 + u][0], wof[ht * 4 + u][1], wof[ht * 4 + u][2], wof[ht * 4 + u][3]}), of[ht], c[u]);
    const unsigned msk = q16 >= 8 ? 0xffffffffu : 0u;
    const u32x4 lo4 = {pack2(c[0][0], c[0][1]), pack2(c[0][2], c[0][3]), pack2(c[1][0], c[1][1]), pack2(c[1][2], c[1][3])};
    const u32x4 hi4 = {pack2(c[2][0], c[2][1]), pack2(c[2][2], c[2][3]), pack2(c[3][0], c[3][1]), pack2(c[3][2], c[3][3])};
    const u32x4 v = (hi4 & msk) | (lo4 & ~msk);
    __builtin_amdgcn_raw_buffer_store_b128(v, rws, sb + wave * 1024 + lane * 16, 0, AUX_SC1);
    const unsigned a = row_sum(add4(v)) + seedA;
    const unsigned x = row_xor(xor4(v)) ^ (seedB + j32 * PHI);
    __builtin_amdgcn_raw_buffer_store_b128(u32x4{a, x, seedA, 0u}, rws, q16 == 0 ? sb + 7168 + (wave * 4 + g4) * 16 : OOB, 0, AUX_SC1);
  }
  if (j32 < 28) {
    const int G = j32 >> 2, g = j32 & 3;
    const unsigned bb = W_H2 + b * 32 * H2_UNIT;
    const unsigned po = bb + (4 * wave + g4) * H2_UNIT + G * 1024 + (g * 16 + q16) * 16;
    const unsigned sx = seed4(seedB + (4 * wave) * PHI);
    const unsigned ca = lane < 4 ? bb + (4 * wave + lane) * H2_UNIT + 7168 + j32 * 16 : OOB;
    __builtin_amdgcn_s_sleep(24);
    u32x4 v = {0u, 0u, 0u, 0u};
    unsigned need = 0xFu, seen = 0u;
    for (unsigned spins = 0;; ++spins) {
      FENCE();
      const u32x4 c = __builtin_amdgcn_raw_buffer_load_b128(rws, ca, 0, AUX_SC1);
      const bool mine = (need >> g4) & 1u;
      const u32x4 n = __builtin_amdgcn_raw_buffer_load_b128(rws, mine ? po : OOB, 0, AUX_SC1);
      const unsigned m0 = mine ? 0xffffffffu : 0u;
      v = (n & m0) | (v & ~m0);
      const unsigned now = (unsigned)__ballot(c.z == seedA) & 0xFu;
      if (now == 0xFu) {
        const unsigned a = wave_sum(add4(v) - c.x);
        const unsigned x = wave_xor(xor4(v) ^ c.y);
        const bool ok = (a == 0u - 4u * seedA) && (x == sx);
        if (__builtin_amdgcn_readfirstlane(ok) || spins > SPIN_CAP) break;
        need = 0xFu;
      } else {
        if (spins > SPIN_CAP) break;
        need = ~(now & seen) & 0xFu;
        __builtin_amdgcn_s_sleep(1);
      }
      seen = now;
    }
    f32x4 s0 = {bflo(v.x), bfhi(v.x), bflo(v.y), bfhi(v.y)}, s1 = {bflo(v.z), bfhi(v.z), bflo(v.w), bfhi(v.w)};
#pragma unroll
    for (int u = 0; u < 4; ++u) { s0[u] = fadd32(fadd16(s0[u])); s1[u] = fadd32(fadd16(s1[u])); }
    if (lane < 16) { *(LAS f32x4*)(smem + L_RED + wave * 512 + lane * 32) = s0; *(LAS f32x4*)(smem + L_RED + wave * 512 + lane * 32 + 16) = s1; }
    BARRIER_LDS();
    if (wave == 0 && lane < 32) {
      LAS char* rp = smem + (L_RED + q16 * 32 + (lane >> 4) * 16);
      f32x4 t = *(LAS f32x4*)rp;
#pragma unroll
      for (int w = 1; w < 8; ++w) t += *(LAS f32x4*)(rp + w * 512);
      const int col = 64 * G + 16 * (2 * (q16 >> 3) + (lane >> 4)) + 4 * g;
      *(GAS u32x2*)(p.out + ((size_t)(b * 8 + (q16 & 7)) * DM + col) * 2) = u32x2{pack2(t[0], t[1]), pack2(t[2], t[3])};
      if (j32 == 0 && (b & 1) == 0 && lane == 0)
        __hip_atomic_store((GAS u64*)(p.ws + W_E + rg * 256), ((u64)tw.y << 32) | tw.x, __ATOMIC_RELAXED, __HIP_MEMORY_SCOPE_AGENT);
    }
  }
}

}

extern "C" void kernel_launch(void* const* d_in, const int* in_sizes, int n_in, void* d_out, int out_size, void* d_ws,
                              size_t ws_size, hipStream_t stream) {
  Params p;
  p.x = (const char*)d_in[0];
  p.kc = (const char*)d_in[1];
  p.vc = (const char*)d_in[2];
  p.wq = (const char*)d_in[3];
  p.wo = (const char*)d_in[4];
  p.out = (char*)d_out;
  p.ws = (char*)d_ws;
  (void)hipFuncSetAttribute((const void*)attn_block_kernel, hipFuncAttributeMaxDynamicSharedMemorySize, L_TOTAL);
  hipLaunchKernelGGL(attn_block_kernel, dim3(256), dim3(512), L_TOTAL, stream, p);
}
```
